```python
import torch

def _softmax(x, axis=-1):
    return torch.softmax(x, dim=axis)
def _gelu(x):
    return torch.nn.functional.gelu(x, approximate="tanh")
def _silu(x):
    return torch.nn.functional.silu(x)
def _sigmoid(x):
    return torch.sigmoid(x)
def _layernorm(x, axis=-1, eps=1e-5):
    m = x.mean(dim=axis, keepdim=True)
    v = x.var(dim=axis, keepdim=True, unbiased=False)
    return (x - m) / torch.sqrt(v + eps)
def _scatter_add(tgt, idx, src, axis=0):
    return tgt.clone().index_add_(axis, idx, src)
def _segment_sum(src, seg, n):
    out = torch.zeros((n,) + src.shape[1:], dtype=src.dtype)
    return out.index_add_(0, seg, src)
def _pad(x, pw):
    flat = tuple(v for pair in reversed(pw) for v in pair)
    return torch.nn.functional.pad(x, flat)

kernel_name = 'gated_mix__linear_attn_block__mixer_token_mlp__prompt'

def setup_inputs():

    _w = (lambda a: (lambda t: t.to(torch.bfloat16) if t.is_floating_point() else t)(torch.as_tensor(a)))
    def _A_setup():
        B, S, D = 1, 2048, 512
        return {
            "x": _w((lambda *s: torch.randn(*s, dtype=torch.bfloat16) * 0.05)(B, S, D)),
            "wqkv": _w((lambda *s: torch.randn(*s, dtype=torch.bfloat16) * 0.4)(D, 3 * D)),
            "wo": _w((lambda *s: torch.randn(*s, dtype=torch.bfloat16) * 0.05)(D, D)),
        }
    _A_in = _A_setup()

    def _B_setup():
        B, S, D, F = 1, 2048, 512, 1792
        return {
            "x": _w((lambda *s: torch.randn(*s, dtype=torch.bfloat16) * 0.05)(B, S, D)),
            "w1": _w((lambda *s: torch.randn(*s, dtype=torch.bfloat16) * 0.4)(S, F) * (S ** -0.5)),
            "b1": _w((lambda *s: torch.randn(*s, dtype=torch.bfloat16) * 3.2)(F) * 0.02),
            "w2": _w((lambda *s: torch.randn(*s, dtype=torch.bfloat16) * 0.05)(F, S) * (F ** -0.5)),
            "b2": _w((lambda *s: torch.randn(*s, dtype=torch.bfloat16) * 0.1)(S) * 0.02),
        }
    _B_in = _B_setup()

    return {
        "A_x": _A_in["x"],
        "A_wqkv": _A_in["wqkv"],
        "A_wo": _A_in["wo"],
        "B_x": _B_in["x"],
        "B_w1": _B_in["w1"],
        "B_b1": _B_in["b1"],
        "B_w2": _B_in["w2"],
        "B_b2": _B_in["b2"],
        "gate_w": _w((lambda *s: torch.randn(*s, dtype=torch.bfloat16) * 0.05)(512) * 16.0),
    }

def reference(A_x, A_wqkv, A_wo, B_x, B_w1, B_b1, B_w2, B_b2, gate_w):
    def _A_ref(x, wqkv, wo):
        B, S, D = x.shape
        H = 32; Dh = D // H
        qkv = (lambda x, *d: x.permute(*d))((x @ wqkv).reshape(B, S, 3, H, Dh), 2, 0, 3, 1, 4)
        q, k, v = qkv[0], qkv[1], qkv[2]
        phi = lambda t: torch.where(t > 0, t + 1, torch.exp(t)) + 1e-6
        pq, pk = phi(q), phi(k)
        kv = torch.einsum("bhsd,bhse->bhde", pk, v)
        z = torch.einsum("bhsd->bhd", pk)
        num = torch.einsum("bhqd,bhde->bhqe", pq, kv)
        den = torch.einsum("bhqd,bhd->bhq", pq, z)[..., None] + 1e-6
        o = num / den
        return (lambda x, *d: x.permute(*d))(o, 0, 2, 1, 3).reshape(B, S, D) @ wo

    def _B_ref(x, w1, b1, w2, b2):
        B, S, D = x.shape
        h = _layernorm(x, axis=-1)
        u = torch.einsum("bsd,sf->bdf", h, w1) + b1
        u = _gelu(u)
        y = torch.einsum("bdf,fs->bds", u, w2) + b2
        return (lambda x, *d: x.permute(*d))(y, 0, 2, 1)

    x = A_x
    g = _sigmoid((x * gate_w).sum(-1, keepdims=True))
    a = _A_ref(x, A_wqkv, A_wo)
    b = _B_ref(x, B_w1, B_b1, B_w2, B_b2)
    return g * a + (1 - g) * b
```

```cpp
#include <hip/hip_runtime.h>
#include <hip/hip_bf16.h>
#include <stdint.h>

typedef unsigned short u16;
__device__ __forceinline__ float bf2f(u16 v){ return __uint_as_float(((uint32_t)v)<<16); }
__device__ __forceinline__ u16 f2bf(float f){ __hip_bfloat16 h = __float2bfloat16(f); return *reinterpret_cast<u16*>(&h); }

constexpr int S=2048, D=512, F=1792, H=32, DH=16;

__global__ void k_ln(const u16* x, const u16* gw, u16* h, float* g){
  int row = blockIdx.x; int lane = threadIdx.x;
  float v[8]; float s=0, gs=0;
  for(int j=0;j<8;j++){ v[j]=bf2f(x[row*D+lane*8+j]); s+=v[j]; gs += bf2f(f2bf(v[j]*bf2f(gw[lane*8+j]))); }
  for(int o=32;o;o>>=1){ s+=__shfl_xor(s,o); gs+=__shfl_xor(gs,o);}
  float m=s/D; float q=0;
  for(int j=0;j<8;j++){ float d=v[j]-m; q+=d*d; }
  for(int o=32;o;o>>=1){ q+=__shfl_xor(q,o);}
  float r = rsqrtf(q/D+1e-5f);
  for(int j=0;j<8;j++) h[row*D+lane*8+j]=f2bf((v[j]-m)*r);
  if(lane==0) g[row]=1.f/(1.f+__expf(-gs));
}
template<int MODE>
__global__ void k_gemm(const u16* A, int sam, int sak, const u16* B, int sbk, int sbn, int M, int N, int K, float* C, const u16* bias){
  int n = blockIdx.x*16+threadIdx.x%16; int m = blockIdx.y*16+threadIdx.x/16;
  float acc=0;
  for(int k=0;k<K;k++) acc += bf2f(A[m*sam+k*sak])*bf2f(B[k*sbk+n*sbn]);
  C[m*N+n]=acc;
}
__global__ void k_phi(const float* qkv, u16* pq, u16* pk, u16* v){
  int i = blockIdx.x*256+threadIdx.x;
  int s=i/D, c=i%D;
  float q=bf2f(f2bf(qkv[s*1536+c])), k=bf2f(f2bf(qkv[s*1536+512+c])), vv=qkv[s*1536+1024+c];
  auto phi=[](float t){ return (t>0? t+1.f : __expf(t)) + 1e-6f; };
  pq[i]=f2bf(phi(q)); pk[i]=f2bf(phi(k)); v[i]=f2bf(vv);
}
__global__ void k_kv(const u16* pk, const u16* v, float* kv, float* z){
  int h=blockIdx.x; int d=threadIdx.x/16, e=threadIdx.x%16;
  float acc=0, zz=0;
  for(int s=0;s<S;s++){ float p=bf2f(pk[s*D+h*16+d]); acc+=p*bf2f(v[s*D+h*16+e]); zz+=p; }
  kv[h*256+d*16+e]=acc; if(e==0) z[h*16+d]=zz;
}
__global__ void k_o(const u16* pq, const float* kv, const float* z, u16* o){
  int i = blockIdx.x*256+threadIdx.x; int s=i/D, c=i%D; int h=c/16, e=c%16;
  float num=0, den=0;
  for(int d=0;d<16;d++){ float p=bf2f(pq[s*D+h*16+d]); num+=p*kv[h*256+d*16+e]; den+=p*z[h*16+d]; }
  o[i]=f2bf(num/(den+1e-6f));
}
__global__ void k_gelu(const float* u, const u16* b1, u16* ub){
  int i = blockIdx.x*256+threadIdx.x; int f=i%F;
  float t=u[i]+bf2f(b1[f]);
  float y=0.5f*t*(1.f+tanhf(0.7978845608f*(t+0.044715f*t*t*t)));
  ub[i]=f2bf(y);
}
__global__ void k_final(const float* a, const float* y, const u16* b2, const float* g, u16* out){
  int i = blockIdx.x*256+threadIdx.x; int s=i/D, d=i%D;
  float gg=g[s];
  out[i]=f2bf(gg*a[i]+(1.f-gg)*(y[d*S+s]+bf2f(b2[s])));
}

extern "C" void kernel_launch(void* const* d_in, const int* in_sizes, int n_in,
                              void* d_out, int out_size, void* d_ws, size_t ws_size,
                              hipStream_t stream) {
  const u16* x=(const u16*)d_in[0]; const u16* wqkv=(const u16*)d_in[1]; const u16* wo=(const u16*)d_in[2];
  const u16* w1=(const u16*)d_in[4]; const u16* b1=(const u16*)d_in[5]; const u16* w2=(const u16*)d_in[6]; const u16* b2=(const u16*)d_in[7]; const u16* gw=(const u16*)d_in[8];
  char* ws=(char*)d_ws;
  u16* h=(u16*)ws; ws+=S*D*2;
  float* g=(float*)ws; ws+=S*4;
  float* qkv=(float*)ws; ws+=S*1536*4;
  u16* pq=(u16*)ws; ws+=S*D*2;
  u16* pk=(u16*)ws; ws+=S*D*2;
  u16* v=(u16*)ws; ws+=S*D*2;
  float* kv=(float*)ws; ws+=32*256*4;
  float* z=(float*)ws; ws+=512*4;
  u16* o=(u16*)ws; ws+=S*D*2;
  float* a=(float*)ws; ws+=S*D*4;
  float* u=(float*)ws; ws+=D*F*4;
  u16* ub=(u16*)ws; ws+=D*F*2;
  float* y=(float*)ws; ws+=D*S*4;
  k_ln<<<S,64,0,stream>>>(x,gw,h,g);
  k_gemm<0><<<dim3(1536/16,S/16),256,0,stream>>>(x,D,1,wqkv,1536,1,S,1536,D,qkv,nullptr);
  k_phi<<<S*D/256,256,0,stream>>>(qkv,pq,pk,v);
  k_kv<<<32,256,0,stream>>>(pk,v,kv,z);
  k_o<<<S*D/256,256,0,stream>>>(pq,kv,z,o);
  k_gemm<0><<<dim3(D/16,S/16),256,0,stream>>>(o,D,1,wo,D,1,S,D,D,a,nullptr);
  k_gemm<0><<<dim3(F/16,D/16),256,0,stream>>>(h,1,D,w1,F,1,D,F,S,u,nullptr);
  k_gelu<<<D*F/256,256,0,stream>>>(u,b1,ub);
  k_gemm<0><<<dim3(S/16,D/16),256,0,stream>>>(ub,F,1,w2,S,1,D,S,F,y,nullptr);
  k_final<<<S*D/256,256,0,stream>>>(a,y,b2,g,(u16*)d_out);
}
```

```cpp
#include <hip/hip_runtime.h>
#include <stdint.h>

typedef unsigned short u16;
typedef unsigned int u32;
typedef unsigned long long u64;
typedef __bf16 bf16x8 __attribute__((ext_vector_type(8)));
typedef __bf16 bf16x2 __attribute__((ext_vector_type(2)));
typedef short s16x4 __attribute__((ext_vector_type(4)));
typedef short s16x8 __attribute__((ext_vector_type(8)));
typedef float f32x2 __attribute__((ext_vector_type(2)));
typedef float f32x4 __attribute__((ext_vector_type(4)));
typedef float f32x16 __attribute__((ext_vector_type(16)));
typedef u32 u32x2 __attribute__((ext_vector_type(2)));
typedef u32 u32x4 __attribute__((ext_vector_type(4)));
typedef __amdgpu_buffer_rsrc_t rsrc_t;
#define DI __device__ __forceinline__
#define LDS __attribute__((address_space(3)))

#define STAMPS 0
#define AUX_W1 0
#define AUX_W2 0
#define ROT 1

constexpr int S = 2048, D = 512, F = 1792;

constexpr u32 OFF_EPOCH = 0;
constexpr u32 OFF_FA = 4096;
constexpr u32 OFF_FQ = OFF_FA + 16384;
constexpr u32 OFF_FU = OFF_FQ + 16384;
constexpr u32 OFF_FM = OFF_FU + 16384;
constexpr u32 OFF_ZF = OFF_FM + 4096;
constexpr u32 OFF_G = 65536;
constexpr u32 OFF_ZP = OFF_G + 8192;
constexpr u32 OFF_KVP = 262144;
constexpr u32 OFF_HT = OFF_KVP + 1048576;
constexpr u32 OFF_PQ = OFF_HT + 2097152;
constexpr u32 OFF_M = OFF_PQ + 2097152;
constexpr u32 OFF_UT = OFF_M + 524288;
constexpr u32 OFF_TS = OFF_UT + 1835008;
constexpr u32 WS_USED = OFF_TS + 2 * 65536;

constexpr u32 L_LN = 131072;
constexpr u32 L_KVR = L_LN + 8704;
constexpr u32 L_MISC = 147456;
constexpr u32 LDS_BYTES = 163840;

DI rsrc_t mkrsrc(const void* p, u32 bytes) { return __builtin_amdgcn_make_buffer_rsrc((void*)p, (short)0, (int)bytes, 0x00020000); }
DI u32 cvtpk(float a, float b) { f32x2 v = {a, b}; bf16x2 r = __builtin_convertvector(v, bf16x2); return __builtin_bit_cast(u32, r); }
DI float blo(u32 v) { return __builtin_bit_cast(float, v << 16); }
DI float bhi(u32 v) { return __builtin_bit_cast(float, v & 0xffff0000u); }
DI float asf(u32 v) { return __builtin_bit_cast(float, v); }
DI u32 asu(float v) { return __builtin_bit_cast(u32, v); }
DI bf16x8 frag(u32x4 v) { return __builtin_bit_cast(bf16x8, v); }
DI f32x16 mfma(bf16x8 a, bf16x8 b, f32x16 c) { return __builtin_amdgcn_mfma_f32_32x32x16_bf16(a, b, c, 0, 0, 0); }

template <int AUX> DI void dma16(u32 m0v, u32 voff, rsrc_t r, u32 soff) {
  if constexpr (AUX == 0) asm volatile("s_mov_b32 m0, %0\n\ts_nop 0\n\tbuffer_load_dwordx4 %1, %2, %3 offen lds" ::"s"(m0v), "v"(voff), "s"(r), "s"(soff) : "memory");
  else if constexpr (AUX == 2) asm volatile("s_mov_b32 m0, %0\n\ts_nop 0\n\tbuffer_load_dwordx4 %1, %2, %3 offen nt lds" ::"s"(m0v), "v"(voff), "s"(r), "s"(soff) : "memory");
  else asm volatile("s_mov_b32 m0, %0\n\ts_nop 0\n\tbuffer_load_dwordx4 %1, %2, %3 offen sc1 lds" ::"s"(m0v), "v"(voff), "s"(r), "s"(soff) : "memory");
}
DI void ld16c(u32x4& d, u32 voff, rsrc_t r, u32 soff) { asm volatile("buffer_load_dwordx4 %0, %1, %2, %3 offen sc1" : "=v"(d) : "v"(voff), "s"(r), "s"(soff)); }
DI void ld16p(u32x4& d, u32 voff, rsrc_t r, u32 soff) { asm volatile("buffer_load_dwordx4 %0, %1, %2, %3 offen" : "=v"(d) : "v"(voff), "s"(r), "s"(soff)); }
DI void ld8c(u32x2& d, u32 voff, rsrc_t r, u32 soff) { asm volatile("buffer_load_dwordx2 %0, %1, %2, %3 offen sc1" : "=v"(d) : "v"(voff), "s"(r), "s"(soff)); }
DI void ld8p(u32x2& d, u32 voff, rsrc_t r, u32 soff) { asm volatile("buffer_load_dwordx2 %0, %1, %2, %3 offen" : "=v"(d) : "v"(voff), "s"(r), "s"(soff)); }
DI void ld4c(u32& d, u32 voff, rsrc_t r, u32 soff) { asm volatile("buffer_load_dword %0, %1, %2, %3 offen sc1" : "=v"(d) : "v"(voff), "s"(r), "s"(soff)); }
DI void ld2p(u32& d, u32 voff, rsrc_t r, u32 soff) { asm volatile("buffer_load_ushort %0, %1, %2, %3 offen" : "=v"(d) : "v"(voff), "s"(r), "s"(soff)); }
DI void st16c(u32x4 v, u32 voff, rsrc_t r, u32 soff) { asm volatile("buffer_store_dwordx4 %0, %1, %2, %3 offen sc1\n\ts_nop 1" ::"v"(v), "v"(voff), "s"(r), "s"(soff) : "memory"); }
DI void st16p(u32x4 v, u32 voff, rsrc_t r, u32 soff) { asm volatile("buffer_store_dwordx4 %0, %1, %2, %3 offen\n\ts_nop 1" ::"v"(v), "v"(voff), "s"(r), "s"(soff) : "memory"); }
DI void st8c(u32x2 v, u32 voff, rsrc_t r, u32 soff) { asm volatile("buffer_store_dwordx2 %0, %1, %2, %3 offen sc1" ::"v"(v), "v"(voff), "s"(r), "s"(soff) : "memory"); }
DI void st4c(u32 v, u32 voff, rsrc_t r, u32 soff) { asm volatile("buffer_store_dword %0, %1, %2, %3 offen sc1" ::"v"(v), "v"(voff), "s"(r), "s"(soff) : "memory"); }
template <int N> DI void waitvm() { asm volatile("s_waitcnt vmcnt(%0)" ::"i"(N) : "memory"); }
#define WAIT_BAR(N) asm volatile("s_waitcnt vmcnt(%0) lgkmcnt(0)\n\ts_barrier" ::"i"(N) : "memory")
DI void lbar() { asm volatile("s_waitcnt lgkmcnt(0)\n\ts_barrier" ::: "memory"); }

DI s16x4 trread(u32 a) { return __builtin_amdgcn_ds_read_tr16_b64_v4i16((LDS s16x4*)(size_t)a); }
DI bf16x8 trfrag(u32 a0, u32 a1) { s16x4 lo = trread(a0), hi = trread(a1); return __builtin_bit_cast(bf16x8, (s16x8)__builtin_shufflevector(lo, hi, 0, 1, 2, 3, 4, 5, 6, 7)); }
DI u32x4 ldsr16(u32 a) { return *(LDS u32x4*)(size_t)a; }
DI f32x4 ldsrf4(u32 a) { return *(LDS f32x4*)(size_t)a; }
DI void ldsw16(u32 a, u32x4 v) { *(LDS u32x4*)(size_t)a = v; }
DI void ldswf4(u32 a, f32x4 v) { *(LDS f32x4*)(size_t)a = v; }

template <int CTRL, int RM> DI float dppadd(float v) { return v + __builtin_bit_cast(float, __builtin_amdgcn_update_dpp(0, __builtin_bit_cast(int, v), CTRL, RM, 0xf, false)); }
DI float wavesum(float v) {
  v = dppadd<0x111, 0xf>(v); v = dppadd<0x112, 0xf>(v); v = dppadd<0x114, 0xf>(v); v = dppadd<0x118, 0xf>(v);
  v = dppadd<0x142, 0xa>(v); v = dppadd<0x143, 0xc>(v);
  return __builtin_bit_cast(float, __builtin_amdgcn_readlane(__builtin_bit_cast(int, v), 63));
}
DI float xor32sum(float v) { auto p = __builtin_amdgcn_permlane32_swap(asu(v), asu(v), false, false); return asf(p[0]) + asf(p[1]); }
DI float expf_(float t) { return __builtin_amdgcn_exp2f(t * 1.44269504089f); }
DI float phi(float t) { return (t > 0.f ? t + 1.f : expf_(t)) + 1e-6f; }
DI float gelu(float t) { float y = 0.7978845608f * (t + 0.044715f * t * t * t); return t * __builtin_amdgcn_rcpf(1.f + __builtin_amdgcn_exp2f(-2.88539008178f * y)); }

DI u64 mixtag(u64 e) { e ^= e >> 29; e *= 0x9E3779B97F4A7C15ull; e ^= e >> 32; e += 0x632BE59BD9B4E019ull; return e; }

DI void poll2(rsrc_t ws, u32 voffA, u32 voffB, u32 tlo, u32 thi) {
  for (int it = 0; it < 400000; ++it) {
    u32x2 a, b;
    asm volatile("buffer_load_dwordx2 %0, %2, %4, 0 offen sc1\n\tbuffer_load_dwordx2 %1, %3, %4, 0 offen sc1\n\ts_waitcnt vmcnt(0)" : "=&v"(a), "=&v"(b) : "v"(voffA), "v"(voffB), "s"(ws) : "memory");
    bool ok = a.x == tlo && a.y == thi && b.x == tlo && b.y == thi;
    if (__all(ok)) break;
    __builtin_amdgcn_s_sleep(1);
  }
}
DI u32 blk_g1(int ti, int tj) { int id = tj * 8 + ti; return (u32)((id % 28) * 8 + id / 28); }
DI u32 blk_qkv(int T, int Hq) { return (u32)((((T & 7) * 4 + (Hq & 3)) * 8) + (T >> 3) * 2 + (Hq >> 2)); }

#define STAMP(i) do {} while (0)

struct Ctx {
  rsrc_t rW1, rW2, rWS;
  int lane, hi, kg, dh;
};

DI void reduce_scatter(const f32x16& a0, const f32x16& a1, int lane, int kg, int dh, float (&v)[8]) {
  lbar();
  u32 wb = (u32)(dh * 32768 + kg * 2048 + lane * 16);
#pragma unroll
  for (int p = 0; p < 4; ++p) {
    const f32x16& a = (p >> 1) ? a1 : a0;
    const int o = (p & 1) * 8;
    ldswf4(wb + p * 8192, f32x4{a[o], a[o + 1], a[o + 2], a[o + 3]});
    ldswf4(wb + p * 8192 + 1024, f32x4{a[o + 4], a[o + 5], a[o + 6], a[o + 7]});
  }
  lbar();
  u32 rb = (u32)(dh * 32768 + kg * 8192 + lane * 16);
  f32x4 s0 = ldsrf4(rb), s1 = ldsrf4(rb + 1024);
#pragma unroll
  for (int k = 1; k < 4; ++k) { s0 += ldsrf4(rb + k * 2048); s1 += ldsrf4(rb + k * 2048 + 1024); }
  v[0] = s0[0]; v[1] = s0[1]; v[2] = s0[2]; v[3] = s0[3]; v[4] = s1[0]; v[5] = s1[1]; v[6] = s1[2]; v[7] = s1[3];
}

__global__ __launch_bounds__(512, 2) void fused_kernel(const u16* __restrict__ x, const u16* __restrict__ wqkv, const u16* __restrict__ wo,
                                                       const u16* __restrict__ w1, const u16* __restrict__ b1, const u16* __restrict__ w2,
                                                       const u16* __restrict__ b2, const u16* __restrict__ gw, u16* __restrict__ out, char* __restrict__ wsp) {
  extern __shared__ __attribute__((aligned(16))) char smem[];
  const int tid = threadIdx.x, lane = tid & 63;
  const int w = __builtin_amdgcn_readfirstlane(tid >> 6);
  const int b = blockIdx.x, xcd = b & 7, r = b >> 3;
  const int hi = lane >> 5, l31 = lane & 31;
  const int blk = (lane >> 4) & 1, q = (lane & 15) >> 2, p = lane & 3;
  STAMP(0);

  const rsrc_t rX = mkrsrc(x, S * D * 2), rWQ = mkrsrc(wqkv, D * 1536 * 2), rWS = mkrsrc(wsp, WS_USED);
  const rsrc_t rW1 = mkrsrc(w1, S * F * 2), rW2 = mkrsrc(w2, F * S * 2);

  u32 cdummy = 0;
  if (w == 7) {
    u64 pc; u32 len;
    asm volatile("s_getpc_b64 %0\n.Lgm_begin:\n\ts_mov_b32 %1, .Lgm_end-.Lgm_begin" : "=s"(pc), "=s"(len));
    const char* cp = (const char*)(pc & ~127ull);
    for (u32 o = lane * 128; o < len; o += 8192) asm volatile("global_load_dword %0, %1, off" : "+v"(cdummy) : "v"(cp + o));
  }

  const int T = 8 * (xcd >> 1) + (r >> 2), Hq = 4 * (xcd & 1) + (r & 3);
  u32x2 ev; u32x4 xr, gv;
  ld8c(ev, 0, rWS, OFF_EPOCH);
  {
    const int row = 64 * T + 8 * Hq + w;
    ld16p(xr, lane * 16, rX, row * 1024);
    ld16p(gv, lane * 16, mkrsrc(gw, D * 2), 0);
  }
  const u32 vx = (u32)((64 * T + 8 * w + (lane >> 3)) * 1024 + (((lane & 7) ^ ((4 * w + (lane >> 4)) & 7)) * 16));
  const u32 vw = (u32)((8 * w + (lane >> 3)) * 3072 + Hq * 128 + (((lane & 7) ^ (((lane >> 4) & 1) << 2)) * 16));
  const int qstart = ROT ? ((r >> 2) + 2 * (r & 3)) & 7 : 0;
  auto qkv_issue = [&](int c) {
    const int cc = (c + qstart) & 7;
    const u32 sl = (u32)((c & 3) * 32768 + w * 1024);
    dma16<0>(sl, vx, rX, cc * 128);
    dma16<0>(sl + 8192, vw, rWQ, cc * 64 * 3072);
    dma16<0>(sl + 16384, vw, rWQ, cc * 64 * 3072 + 1024);
    dma16<0>(sl + 24576, vw, rWQ, cc * 64 * 3072 + 2048);
  };
  qkv_issue(0); qkv_issue(1); qkv_issue(2); qkv_issue(3);
  asm volatile("s_waitcnt vmcnt(16)" : "+v"(ev), "+v"(xr), "+v"(gv), "+v"(cdummy)::"memory");
  const u64 tag = mixtag(((u64)(u32)__builtin_amdgcn_readfirstlane(ev.y) << 32) | (u32)__builtin_amdgcn_readfirstlane(ev.x));
  const u32 tlo = (u32)tag, thi = (u32)(tag >> 32);
  const u32x2 tagv = {tlo, thi};
  {
    float v[8] = {blo(xr.x), bhi(xr.x), blo(xr.y), bhi(xr.y), blo(xr.z), bhi(xr.z), blo(xr.w), bhi(xr.w)};
    float gq[8] = {blo(gv.x), bhi(gv.x), blo(gv.y), bhi(gv.y), blo(gv.z), bhi(gv.z), blo(gv.w), bhi(gv.w)};
    float s = 0.f, gs = 0.f;
#pragma unroll
    for (int j = 0; j < 8; ++j) { s += v[j]; gs += v[j] * gq[j]; }
    const float mean = wavesum(s) * (1.f / 512.f);
    gs = wavesum(gs);
    float ss = 0.f;
#pragma unroll
    for (int j = 0; j < 8; ++j) { v[j] -= mean; ss += v[j] * v[j]; }
    const float rstd = __builtin_amdgcn_rsqf(wavesum(ss) * (1.f / 512.f) + 1e-5f);
    u32x4 h = {cvtpk(v[0] * rstd, v[1] * rstd), cvtpk(v[2] * rstd, v[3] * rstd), cvtpk(v[4] * rstd, v[5] * rstd), cvtpk(v[6] * rstd, v[7] * rstd)};
    ldsw16(L_LN + w * 1088 + lane * 16, h);
    lbar();
    const u32 la = L_LN + (u32)(q * 1088 + ((tid >> 4) * 16 + 4 * p) * 2);
    bf16x8 ht = trfrag(la, la + 4 * 1088);
    st16c(__builtin_bit_cast(u32x4, ht), tid * 16, rWS, OFF_HT + (8 * T + Hq) * 8192);
    const float g = __builtin_amdgcn_rcpf(1.f + expf_(-gs));
    if (lane == 0) st4c(asu(g), 0, rWS, OFF_G + (64 * T + 8 * Hq + w) * 4);
  }
  STAMP(1);
  const int th = w & 1, hp = (w >> 1) & 1;
  f32x16 accq = {}, acck = {}, accv = {};
  u32 ax[4];
  {
    const int row = 32 * th + l31, f = (row >> 1) & 7;
#pragma unroll
    for (int ks = 0; ks < 4; ++ks) ax[ks] = (u32)(row * 128 + (((2 * ks + hi) ^ f) * 16));
  }
  const u32 aw = (u32)(8192 + (8 * hi + q) * 128 + (((4 * hp + 2 * blk + (p >> 1)) ^ (((q >> 1) & 1) << 2)) * 16) + 8 * (p & 1));
  auto qkv_comp = [&](int c) {
    if (w < 4) {
      const u32 sl = (u32)((c & 3) * 32768);
#pragma unroll
      for (int ks = 0; ks < 4; ++ks) {
        const bf16x8 xf = frag(ldsr16(sl + ax[ks]));
        const u32 a = sl + aw + ks * 2048;
        const bf16x8 fq = trfrag(a, a + 512), fk = trfrag(a + 8192, a + 8192 + 512), fv = trfrag(a + 16384, a + 16384 + 512);
        accq = mfma(fq, xf, accq);
        acck = mfma(xf, fk, acck);
        accv = mfma(xf, fv, accv);
      }
    }
  };
  WAIT_BAR(14); qkv_comp(0);
  WAIT_BAR(10); qkv_issue(4); qkv_comp(1);
  WAIT_BAR(10); qkv_issue(5); qkv_comp(2);
  WAIT_BAR(10); qkv_issue(6); qkv_comp(3);
  WAIT_BAR(8); qkv_issue(7);
  if (tid == 0) st8c(tagv, 0, rWS, OFF_FA + b * 64);
  qkv_comp(4);
  WAIT_BAR(8); qkv_comp(5);
  WAIT_BAR(4); qkv_comp(6);
  WAIT_BAR(0);
  if (w == 4) poll2(rWS, OFF_FA + lane * 256, OFF_FA + lane * 256 + 64, tlo, thi), poll2(rWS, OFF_FA + lane * 256 + 128, OFF_FA + lane * 256 + 192, tlo, thi);
  qkv_comp(7);
  STAMP(2);
  u32 pq[8];
  {
    f32x16 kvacc = {};
    float zs = 0.f;
    if (w < 4) {
      u32 pk[8], vv[8];
#pragma unroll
      for (int i = 0; i < 8; ++i) {
        const u32 kb = cvtpk(acck[2 * i], acck[2 * i + 1]);
        pk[i] = cvtpk(phi(blo(kb)), phi(bhi(kb)));
        zs += blo(pk[i]) + bhi(pk[i]);
        vv[i] = cvtpk(accv[2 * i], accv[2 * i + 1]);
        const u32 qb = cvtpk(accq[2 * i], accq[2 * i + 1]);
        pq[i] = cvtpk(phi(blo(qb)), phi(bhi(qb)));
      }
      kvacc = mfma(frag(u32x4{pk[0], pk[1], pk[2], pk[3]}), frag(u32x4{vv[0], vv[1], vv[2], vv[3]}), kvacc);
      kvacc = mfma(frag(u32x4{pk[4], pk[5], pk[6], pk[7]}), frag(u32x4{vv[4], vv[5], vv[6], vv[7]}), kvacc);
      zs = xor32sum(zs);
    } else {
#pragma unroll
      for (int i = 0; i < 8; ++i) pq[i] = 0;
    }
    f32x4 k0, k1;
#pragma unroll
    for (int i = 0; i < 4; ++i) { k0[i] = blk ? kvacc[8 + i] : kvacc[i]; k1[i] = blk ? kvacc[12 + i] : kvacc[4 + i]; }
    const u32 sc = L_KVR + hp * 2304;
    if (w < 4 && th == 1) {
      ldswf4(sc + lane * 32, k0); ldswf4(sc + lane * 32 + 16, k1);
      if (lane < 32) *(LDS float*)(size_t)(sc + 2048 + lane * 4) = zs;
    }
    lbar();
    if (w < 4 && th == 0) {
      k0 += ldsrf4(sc + lane * 32); k1 += ldsrf4(sc + lane * 32 + 16);
      zs += *(LDS float*)(size_t)(sc + 2048 + l31 * 4);
      const int head = 4 * Hq + 2 * hp + blk;
      const u32 vo = (u32)(head * 32768 + (hi * 16 + (lane & 15)) * 32);
      st16c(__builtin_bit_cast(u32x4, k0), vo, rWS, OFF_KVP + T * 1024);
      st16c(__builtin_bit_cast(u32x4, k1), vo + 16, rWS, OFF_KVP + T * 1024);
      if (lane < 32) st4c(asu(zs), lane * 4, rWS, OFF_ZP + (Hq * 32 + T) * 256 + hp * 128);
    }
  }
  STAMP(3);

  const int kg = w & 3, dh = w >> 2;
  const int st = xcd * 4 + (r >> 3), dt = r & 7;
  u32 gS0, gS1, b2S0, b2S1, gT;
  ld4c(gS0, (64 * st + l31) * 4, rWS, OFF_G);
  ld4c(gS1, (64 * st + 32 + l31) * 4, rWS, OFF_G);
  ld4c(gT, (64 * T + 32 * th + l31) * 4, rWS, OFF_G);
  {
    const rsrc_t rB2 = mkrsrc(b2, S * 2);
    ld2p(b2S0, (64 * st + l31) * 2, rB2, 0);
    ld2p(b2S1, (64 * st + 32 + l31) * 2, rB2, 0);
  }
  const u32 swz = (u32)(((lane & 7) ^ (((lane >> 4) & 1) << 2)) * 16);
  const u32 vW2 = (u32)((8 * dh + (lane >> 3)) * 4096 + st * 128) + swz;
  const int sC = ROT ? (((r & 7) * 14) >> 3) : 0;
  auto w2_issue = [&](int j) {
    int jj = j + sC; if (jj >= 14) jj -= 14;
    const u32 sl = (u32)(((j + 4) % 9) * 16384 + kg * 4096 + dh * 1024);
    const u32 so = (u32)((448 * kg + 32 * jj) * 4096);
    dma16<AUX_W2>(sl, vW2, rW2, so);
    dma16<AUX_W2>(sl + 2048, vW2, rW2, so + 16 * 4096);
  };
  u32x2 zg;
  if (b == 0 && tid == 0) st8c(tagv, 0, rWS, OFF_EPOCH);

  if (r < 28) {
    const int id = xcd * 28 + r, ti = id & 7, tj = id >> 3;
    u32x2 b1a, b1b;
    {
      const rsrc_t rB1 = mkrsrc(b1, F * 2);
      const u32 f0 = (u32)(64 * tj + 32 * (kg >> 1) + 16 * (kg & 1) + 4 * hi) * 2;
      ld8p(b1a, f0, rB1, 0); ld8p(b1b, f0 + 16, rB1, 0);
    }
    const u32 vW1 = (u32)((8 * dh + (lane >> 3)) * 3584 + tj * 128) + swz;
    const u32 vH = (u32)(hi * 8192 + (64 * ti + 32 * dh + l31) * 16);
    const int sB = ROT ? ((2 * ti + 4 * (tj & 3) + ((tj >> 2) & 1)) & 15) : 0;
    u32x4 hr[4][2];
    auto g1_issue = [&](int s, u32x4 (&h)[2]) {
      const int ss = (s + sB) & 15;
      const u32 sl = (u32)((s & 3) * 16384 + kg * 4096 + dh * 1024);
      const u32 so = (u32)((512 * kg + 32 * ss) * 3584);
      dma16<AUX_W1>(sl, vW1, rW1, so);
      dma16<AUX_W1>(sl + 2048, vW1, rW1, so + 16 * 3584);
      const u32 sh = (u32)(OFF_HT + (32 * kg + 2 * ss) * 16384);
      ld16c(h[0], vH, rWS, sh);
      ld16c(h[1], vH, rWS, sh + 16384);
    };
    const u32 aA0 = (u32)(kg * 4096 + (8 * hi + q) * 128 + (((2 * blk + (p >> 1)) ^ (((q >> 1) & 1) << 2)) * 16) + 8 * (p & 1));
    const u32 aA1 = aA0 ^ 64u;
    f32x16 a0 = {}, a1 = {};
    g1_issue(0, hr[0]); g1_issue(1, hr[1]); g1_issue(2, hr[2]);
    STAMP(4);
#define G1_STAGE(SI)                                                                                              \
  {                                                                                                               \
    asm volatile("s_waitcnt vmcnt(8) lgkmcnt(0)\n\ts_barrier" : "+v"(hr[(SI) & 3][0]), "+v"(hr[(SI) & 3][1])::"memory"); \
    if ((SI) == 0 && tid == 0) st8c(tagv, 0, rWS, OFF_FQ + b * 64);                                               \
    if ((SI) + 3 < 16) g1_issue((SI) + 3, hr[((SI) + 3) & 3]);                                                    \
    else if ((SI) < 15) { w2_issue(2 * ((SI) - 13)); w2_issue(2 * ((SI) - 13) + 1); }                             \
    else w2_issue(4);                                                                                             \
    if ((SI) == 12) ld8c(zg, l31 * 8, rWS, OFF_ZF + (4 * Hq + 2 * hp) * 128);                                     \
    _Pragma("unroll") for (int ks = 0; ks < 2; ++ks) {                                                            \
      const u32 o = (u32)(((SI) & 3) * 16384 + ks * 2048);                                                        \
      const bf16x8 f0 = trfrag(aA0 + o, aA0 + o + 512), f1 = trfrag(aA1 + o, aA1 + o + 512);                      \
      a0 = mfma(f0, frag(hr[(SI) & 3][ks]), a0);                                                                  \
      a1 = mfma(f1, frag(hr[(SI) & 3][ks]), a1);                                                                  \
    }                                                                                                             \
  }
    G1_STAGE(0) G1_STAGE(1) G1_STAGE(2) G1_STAGE(3) G1_STAGE(4) G1_STAGE(5) G1_STAGE(6) G1_STAGE(7)
    G1_STAGE(8) G1_STAGE(9) G1_STAGE(10) G1_STAGE(11) G1_STAGE(12) G1_STAGE(13) G1_STAGE(14) G1_STAGE(15)
    STAMP(5);
    float v[8];
    reduce_scatter(a0, a1, lane, kg, dh, v);
    asm volatile("" : "+v"(b1a), "+v"(b1b));
    const float bb[8] = {blo(b1a.x), bhi(b1a.x), blo(b1a.y), bhi(b1a.y), blo(b1b.x), bhi(b1b.x), blo(b1b.y), bhi(b1b.y)};
    u32x4 uo;
    uo.x = cvtpk(gelu(v[0] + bb[0]), gelu(v[1] + bb[1])); uo.y = cvtpk(gelu(v[2] + bb[2]), gelu(v[3] + bb[3]));
    uo.z = cvtpk(gelu(v[4] + bb[4]), gelu(v[5] + bb[5])); uo.w = cvtpk(gelu(v[6] + bb[6]), gelu(v[7] + bb[7]));
    st16c(uo, vH, rWS, OFF_UT + (4 * tj + kg) * 16384);
  } else {
    const int h = xcd * 4 + (r - 28);
    const rsrc_t rWO = mkrsrc(wo, D * D * 2);
    dma16<0>((u32)(2 * w * 1024), lane * 16, rWO, (16 * h + 2 * w) * 1024);
    dma16<0>((u32)((2 * w + 1) * 1024), lane * 16, rWO, (16 * h + 2 * w + 1) * 1024);
    w2_issue(0); w2_issue(1); w2_issue(2); w2_issue(3); w2_issue(4);
    waitvm<10>();
    lbar();
    if (tid == 0) st8c(tagv, 0, rWS, OFF_FQ + b * 64);
    if (w == 0) { const u32 fo = OFF_FQ + blk_qkv(l31, h >> 2) * 64; poll2(rWS, fo, fo, tlo, thi); }
    lbar();
    u32x4 k4[4];
#pragma unroll
    for (int i = 0; i < 4; ++i) ld16c(k4[i], lane * 16, rWS, OFF_KVP + h * 32768 + (4 * w + i) * 1024);
    u32x4 z4[2];
#pragma unroll
    for (int i = 0; i < 2; ++i) ld16c(z4[i], ((lane >> 2) + 16 * i) * 256 + (lane & 3) * 16, rWS, OFF_ZP + (h >> 2) * 8192 + (h & 3) * 64);
    asm volatile("s_waitcnt vmcnt(0)" : "+v"(k4[0]), "+v"(k4[1]), "+v"(k4[2]), "+v"(k4[3]), "+v"(z4[0]), "+v"(z4[1])::"memory");
    f32x4 ks = __builtin_bit_cast(f32x4, k4[0]) + __builtin_bit_cast(f32x4, k4[1]) + __builtin_bit_cast(f32x4, k4[2]) + __builtin_bit_cast(f32x4, k4[3]);
    ldswf4(16384 + w * 1024 + lane * 16, ks);
    if (w == 0) {
      f32x4 zz = __builtin_bit_cast(f32x4, z4[0]) + __builtin_bit_cast(f32x4, z4[1]);
#pragma unroll
      for (int i = 0; i < 4; ++i) {
        float t = zz[i];
        t += __shfl_xor(t, 4); t += __shfl_xor(t, 8); t += __shfl_xor(t, 16); t += __shfl_xor(t, 32);
        zz[i] = t;
      }
      if (lane < 4) ldswf4(25600 + lane * 16, zz);
    }
    lbar();
    if (w == 0) {
      f32x4 t = ldsrf4(16384 + lane * 16);
#pragma unroll
      for (int i = 1; i < 8; ++i) t += ldsrf4(16384 + i * 1024 + lane * 16);
      const int half = lane & 1, e = (lane >> 1) & 15, hl = lane >> 5;
      const u32 pk2 = cvtpk(t[0], t[1]), pk3 = cvtpk(t[2], t[3]);
      const u32 o = 24576 + (8 * half + 4 * hl) * 32 + e * 2;
      *(LDS u16*)(size_t)(o) = (u16)pk2; *(LDS u16*)(size_t)(o + 32) = (u16)(pk2 >> 16);
      *(LDS u16*)(size_t)(o + 64) = (u16)pk3; *(LDS u16*)(size_t)(o + 96) = (u16)(pk3 >> 16);
      if (lane < 16) { const float z = *(LDS float*)(size_t)(25600 + lane * 4); st8c(u32x2{asu(z), tlo}, lane * 8, rWS, OFF_ZF + h * 128); }
    }
    lbar();
    u32x4 kf = ldsr16(24576 + (l31 & 15) * 32 + hi * 16);
    if (l31 >= 16) kf = u32x4{0, 0, 0, 0};
#pragma unroll
    for (int i = 0; i < 2; ++i) {
      const int dtile = 2 * w + i;
      const u32 a = (u32)((8 * hi + q) * 1024 + dtile * 64 + (2 * blk + (p >> 1)) * 16 + 8 * (p & 1));
      const bf16x8 fo = trfrag(a, a + 4096);
      f32x16 m = {};
      m = mfma(frag(kf), fo, m);
      st16c(u32x4{cvtpk(m[0], m[1]), cvtpk(m[2], m[3]), cvtpk(m[4], m[5]), cvtpk(m[6], m[7])}, lane * 16, rWS, OFF_M + (h * 16 + dtile) * 1024);
    }
    waitvm<0>();
    lbar();
    if (tid == 0) st8c(tagv, 0, rWS, OFF_FM + h * 64);
    ld8c(zg, l31 * 8, rWS, OFF_ZF + (4 * Hq + 2 * hp) * 128);
  }
  asm volatile("s_waitcnt vmcnt(0)" : "+v"(zg), "+v"(gT), "+v"(gS0), "+v"(gS1), "+v"(b2S0), "+v"(b2S1)::"memory");
  if (w < 4) {
    for (int it = 0; it < 400000; ++it) {
      if (__all(zg.y == tlo)) break;
      __builtin_amdgcn_s_sleep(1);
      asm volatile("buffer_load_dwordx2 %0, %1, %2, %3 offen sc1\n\ts_waitcnt vmcnt(0)" : "=&v"(zg) : "v"(l31 * 8), "s"(rWS), "s"((u32)(OFF_ZF + (4 * Hq + 2 * hp) * 128)) : "memory");
    }
    const u32 zs = L_MISC + w * 128;
    if (lane < 32) *(LDS u32*)(size_t)(zs + lane * 4) = zg.x;
    asm volatile("s_waitcnt lgkmcnt(0)" ::: "memory");
#pragma unroll
    for (int par = 0; par < 2; ++par) {
      const f32x4 z0 = ldsrf4(zs + par * 64 + hi * 16), z1 = ldsrf4(zs + par * 64 + 32 + hi * 16);
      float e[8];
#pragma unroll
      for (int i = 0; i < 4; ++i) { e[2 * i] = blo(pq[4 * par + i]); e[2 * i + 1] = bhi(pq[4 * par + i]); }
      float den = e[0] * z0[0] + e[1] * z0[1] + e[2] * z0[2] + e[3] * z0[3] + e[4] * z1[0] + e[5] * z1[1] + e[6] * z1[2] + e[7] * z1[3];
      den = xor32sum(den) + 1e-6f;
      const float sc = asf(gT) * __builtin_amdgcn_rcpf(den);
      st16c(u32x4{cvtpk(e[0] * sc, e[1] * sc), cvtpk(e[2] * sc, e[3] * sc), cvtpk(e[4] * sc, e[5] * sc), cvtpk(e[6] * sc, e[7] * sc)}, lane * 16, rWS,
            OFF_PQ + ((4 * Hq + 2 * hp + par) * 64 + 2 * T + th) * 1024);
    }
  }
  waitvm<0>();
  lbar();
  if (tid == 0) st8c(tagv, 0, rWS, OFF_FU + b * 64);
  STAMP(6);

  w2_issue(5); w2_issue(6); w2_issue(7); w2_issue(8);
  if (w == 0) {
    const u32 fa = OFF_FU + (lane < 28 ? blk_g1(dt, lane) : blk_qkv(st, lane & 7)) * 64;
    poll2(rWS, fa, OFF_FM + l31 * 64, tlo, thi);
  }
  lbar();
  STAMP(7);
  {
    const u32 vU = (u32)(hi * 8192 + (64 * dt + 32 * dh + l31) * 16);
    const u32 aB0 = (u32)(kg * 4096 + (4 * hi + q) * 128 + (((2 * blk + (p >> 1)) ^ (((q >> 1) & 1) << 2)) * 16) + 8 * (p & 1));
    const u32 aB1 = aB0 ^ 64u;
    u32x4 ur[6][2];
    auto dir_issue = [&](int j, u32x4 (&u)[2]) {
      if (j < 14) {
        int jj = j + sC; if (jj >= 14) jj -= 14;
        const u32 so = (u32)(OFF_UT + (28 * kg + 2 * jj) * 16384);
        ld16c(u[0], vU, rWS, so); ld16c(u[1], vU, rWS, so + 16384);
      } else {
        const u32 so = (u32)(OFF_M + ((8 * kg + 2 * (j - 14)) * 16 + 2 * dt + dh) * 1024);
        ld16c(u[0], lane * 16, rWS, so); ld16c(u[1], lane * 16, rWS, so + 16384);
      }
    };
    auto lds_issue = [&](int j) {
      if (j < 14) w2_issue(j);
      else {
        const u32 sl = (u32)(((j + 4) % 9) * 16384 + kg * 4096 + dh * 1024);
        const u32 so = (u32)(OFF_PQ + ((8 * kg + 2 * (j - 14)) * 64 + 2 * st + dh) * 1024);
        dma16<16>(sl, lane * 16, rWS, so); dma16<16>(sl + 2048, lane * 16, rWS, so + 65536);
      }
    };
    f32x16 a0 = {}, a1 = {};
    dir_issue(0, ur[0]); dir_issue(1, ur[1]); dir_issue(2, ur[2]); dir_issue(3, ur[3]); dir_issue(4, ur[4]);
#define C_OPS(i) (((i) >= 1 && (i) <= 9 ? 2 : 0) + ((i) >= 0 && (i) <= 12 ? 2 : 0))
#define C_MIN(a, b) ((a) < (b) ? (a) : (b))
#define C_ND(J) (((J) < 5 ? 2 * (4 - (J)) : 0) + C_OPS((J)-4) + C_OPS((J)-3) + C_OPS((J)-2) + C_OPS((J)-1))
#define C_NL(J) ((J) < 9 ? 63 : 2 + C_OPS((J)-7) + C_OPS((J)-6) + C_OPS((J)-5) + C_OPS((J)-4) + C_OPS((J)-3) + C_OPS((J)-2) + C_OPS((J)-1))
#define C_STAGE(J)                                                                                                \
  {                                                                                                               \
    asm volatile("s_waitcnt vmcnt(%2) lgkmcnt(0)\n\ts_barrier" : "+v"(ur[(J) % 6][0]), "+v"(ur[(J) % 6][1]) : "i"(C_MIN(C_ND(J), C_NL(J))) : "memory"); \
    if ((J) >= 1 && (J) <= 9) lds_issue((J) + 8);                                                                 \
    if ((J) <= 12) dir_issue((J) + 5, ur[((J) + 5) % 6]);                                                         \
    if ((J) == 14) { const float s0 = 1.f - asf(gS0), s1 = 1.f - asf(gS1); a0 *= s0; a1 *= s1; }                  \
    _Pragma("unroll") for (int ks = 0; ks < 2; ++ks) {                                                            \
      const u32 o = (u32)((((J) + 4) % 9) * 16384 + ks * 2048);                                                   \
      bf16x8 f0, f1;                                                                                              \
      if ((J) < 14) { f0 = trfrag(aB0 + o, aB0 + o + 1024); f1 = trfrag(aB1 + o, aB1 + o + 1024); }               \
      else { f0 = frag(ldsr16(o + kg * 4096 + lane * 16)); f1 = frag(ldsr16(o + kg * 4096 + 1024 + lane * 16)); } \
      a0 = mfma(frag(ur[(J) % 6][ks]), f0, a0);                                                                   \
      a1 = mfma(frag(ur[(J) % 6][ks]), f1, a1);                                                                   \
    }                                                                                                             \
  }
    C_STAGE(0) C_STAGE(1) C_STAGE(2) C_STAGE(3) C_STAGE(4) C_STAGE(5) C_STAGE(6) C_STAGE(7) C_STAGE(8)
    C_STAGE(9) C_STAGE(10) C_STAGE(11) C_STAGE(12) C_STAGE(13) C_STAGE(14) C_STAGE(15) C_STAGE(16) C_STAGE(17)
    STAMP(8);
    float v[8];
    reduce_scatter(a0, a1, lane, kg, dh, v);
    const float gs = (kg >> 1) ? asf(gS1) : asf(gS0);
    const float bias = (1.f - gs) * blo((kg >> 1) ? b2S1 : b2S0);
    u32 a_x = cvtpk(v[0] + bias, v[1] + bias), a_y = cvtpk(v[2] + bias, v[3] + bias), b_x = cvtpk(v[4] + bias, v[5] + bias), b_y = cvtpk(v[6] + bias, v[7] + bias);
    auto s0 = __builtin_amdgcn_permlane32_swap(a_x, b_x, false, false);
    auto s1 = __builtin_amdgcn_permlane32_swap(a_y, b_y, false, false);
    const rsrc_t rO = mkrsrc(out, S * D * 2);
    st16p(u32x4{s0[0], s1[0], s0[1], s1[1]}, (u32)((64 * st + 32 * (kg >> 1) + l31) * 1024 + (64 * dt + 32 * dh + 16 * (kg & 1) + 8 * hi) * 2), rO, 0);
  }
  STAMP(9);
  asm volatile(".Lgm_end:" ::: "memory");
}

extern "C" void kernel_launch(void* const* d_in, const int* in_sizes, int n_in, void* d_out, int out_size, void* d_ws, size_t ws_size, hipStream_t stream) {
  (void)in_sizes; (void)n_in; (void)out_size; (void)ws_size;
  (void)hipFuncSetAttribute((const void*)fused_kernel, hipFuncAttributeMaxDynamicSharedMemorySize, LDS_BYTES);
  fused_kernel<<<256, 512, LDS_BYTES, stream>>>((const u16*)d_in[0], (const u16*)d_in[1], (const u16*)d_in[2], (const u16*)d_in[4], (const u16*)d_in[5],
                                               (const u16*)d_in[6], (const u16*)d_in[7], (const u16*)d_in[8], (u16*)d_out, (char*)d_ws);
}
```

```cpp
#include <hip/hip_runtime.h>
#include <stdint.h>

typedef unsigned short u16;
typedef unsigned int u32;
typedef unsigned long long u64;
typedef __bf16 bf16x8 __attribute__((ext_vector_type(8)));
typedef __bf16 bf16x2 __attribute__((ext_vector_type(2)));
typedef short s16x4 __attribute__((ext_vector_type(4)));
typedef short s16x8 __attribute__((ext_vector_type(8)));
typedef float f32x2 __attribute__((ext_vector_type(2)));
typedef float f32x4 __attribute__((ext_vector_type(4)));
typedef float f32x16 __attribute__((ext_vector_type(16)));
typedef u32 u32x2 __attribute__((ext_vector_type(2)));
typedef u32 u32x4 __attribute__((ext_vector_type(4)));
typedef __amdgpu_buffer_rsrc_t rsrc_t;
#define DI __device__ __forceinline__
#define LDS __attribute__((address_space(3)))

#define STAMPS 0
#define AUX_W1 0
#define AUX_W2 0
#define ROT 1

constexpr int S = 2048, D = 512, F = 1792;

constexpr u32 OFF_EPOCH = 0;
constexpr u32 OFF_FA = 4096;
constexpr u32 OFF_FQ = OFF_FA + 16384;
constexpr u32 OFF_FU = OFF_FQ + 16384;
constexpr u32 OFF_FM = OFF_FU + 16384;
constexpr u32 OFF_ZF = OFF_FM + 4096;
constexpr u32 OFF_G = 65536;
constexpr u32 OFF_ZP = OFF_G + 8192;
constexpr u32 OFF_KVP = 262144;
constexpr u32 OFF_HT = OFF_KVP + 1048576;
constexpr u32 OFF_PQ = OFF_HT + 2097152;
constexpr u32 OFF_M = OFF_PQ + 2097152;
constexpr u32 OFF_UT = OFF_M + 524288;
constexpr u32 OFF_TS = OFF_UT + 1835008;
constexpr u32 WS_USED = OFF_TS + 4 * 65536;

constexpr u32 L_LN = 131072;
constexpr u32 L_KVR = L_LN + 8704;
constexpr u32 L_MISC = 147456;
constexpr u32 LDS_BYTES = 163840;

DI rsrc_t mkrsrc(const void* p, u32 bytes) { return __builtin_amdgcn_make_buffer_rsrc((void*)p, (short)0, (int)bytes, 0x00020000); }
DI u32 cvtpk(float a, float b) { f32x2 v = {a, b}; bf16x2 r = __builtin_convertvector(v, bf16x2); return __builtin_bit_cast(u32, r); }
DI float blo(u32 v) { return __builtin_bit_cast(float, v << 16); }
DI float bhi(u32 v) { return __builtin_bit_cast(float, v & 0xffff0000u); }
DI float asf(u32 v) { return __builtin_bit_cast(float, v); }
DI u32 asu(float v) { return __builtin_bit_cast(u32, v); }
DI bf16x8 frag(u32x4 v) { return __builtin_bit_cast(bf16x8, v); }
DI f32x16 mfma(bf16x8 a, bf16x8 b, f32x16 c) { return __builtin_amdgcn_mfma_f32_32x32x16_bf16(a, b, c, 0, 0, 0); }

template <int AUX> DI void dma16(u32 m0v, u32 voff, rsrc_t r, u32 soff) {
  if constexpr (AUX == 0) asm volatile("s_mov_b32 m0, %0\n\ts_nop 0\n\tbuffer_load_dwordx4 %1, %2, %3 offen lds" ::"s"(m0v), "v"(voff), "s"(r), "s"(soff) : "memory");
  else if constexpr (AUX == 2) asm volatile("s_mov_b32 m0, %0\n\ts_nop 0\n\tbuffer_load_dwordx4 %1, %2, %3 offen nt lds" ::"s"(m0v), "v"(voff), "s"(r), "s"(soff) : "memory");
  else asm volatile("s_mov_b32 m0, %0\n\ts_nop 0\n\tbuffer_load_dwordx4 %1, %2, %3 offen sc1 lds" ::"s"(m0v), "v"(voff), "s"(r), "s"(soff) : "memory");
}
DI void ld16c(u32x4& d, u32 voff, rsrc_t r, u32 soff) { asm volatile("buffer_load_dwordx4 %0, %1, %2, %3 offen sc1" : "=v"(d) : "v"(voff), "s"(r), "s"(soff)); }
DI void ld16p(u32x4& d, u32 voff, rsrc_t r, u32 soff) { asm volatile("buffer_load_dwordx4 %0, %1, %2, %3 offen" : "=v"(d) : "v"(voff), "s"(r), "s"(soff)); }
DI void ld8c(u32x2& d, u32 voff, rsrc_t r, u32 soff) { asm volatile("buffer_load_dwordx2 %0, %1, %2, %3 offen sc1" : "=v"(d) : "v"(voff), "s"(r), "s"(soff)); }
DI void ld8p(u32x2& d, u32 voff, rsrc_t r, u32 soff) { asm volatile("buffer_load_dwordx2 %0, %1, %2, %3 offen" : "=v"(d) : "v"(voff), "s"(r), "s"(soff)); }
DI void ld4c(u32& d, u32 voff, rsrc_t r, u32 soff) { asm volatile("buffer_load_dword %0, %1, %2, %3 offen sc1" : "=v"(d) : "v"(voff), "s"(r), "s"(soff)); }
DI void ld2p(u32& d, u32 voff, rsrc_t r, u32 soff) { asm volatile("buffer_load_ushort %0, %1, %2, %3 offen" : "=v"(d) : "v"(voff), "s"(r), "s"(soff)); }
DI void st16c(u32x4 v, u32 voff, rsrc_t r, u32 soff) { asm volatile("buffer_store_dwordx4 %0, %1, %2, %3 offen sc1\n\ts_nop 1" ::"v"(v), "v"(voff), "s"(r), "s"(soff) : "memory"); }
DI void st16p(u32x4 v, u32 voff, rsrc_t r, u32 soff) { asm volatile("buffer_store_dwordx4 %0, %1, %2, %3 offen\n\ts_nop 1" ::"v"(v), "v"(voff), "s"(r), "s"(soff) : "memory"); }
DI void st8c(u32x2 v, u32 voff, rsrc_t r, u32 soff) { asm volatile("buffer_store_dwordx2 %0, %1, %2, %3 offen sc1" ::"v"(v), "v"(voff), "s"(r), "s"(soff) : "memory"); }
DI void st4c(u32 v, u32 voff, rsrc_t r, u32 soff) { asm volatile("buffer_store_dword %0, %1, %2, %3 offen sc1" ::"v"(v), "v"(voff), "s"(r), "s"(soff) : "memory"); }
template <int N> DI void waitvm() { asm volatile("s_waitcnt vmcnt(%0)" ::"i"(N) : "memory"); }
#define WAIT_BAR(N) asm volatile("s_waitcnt vmcnt(%0) lgkmcnt(0)\n\ts_barrier" ::"i"(N) : "memory")
DI void lbar() { asm volatile("s_waitcnt lgkmcnt(0)\n\ts_barrier" ::: "memory"); }

DI s16x4 trread(u32 a) { return __builtin_amdgcn_ds_read_tr16_b64_v4i16((LDS s16x4*)(size_t)a); }
DI bf16x8 trfrag(u32 a0, u32 a1) { s16x4 lo = trread(a0), hi = trread(a1); return __builtin_bit_cast(bf16x8, (s16x8)__builtin_shufflevector(lo, hi, 0, 1, 2, 3, 4, 5, 6, 7)); }
DI u32x4 ldsr16(u32 a) { return *(LDS u32x4*)(size_t)a; }
DI f32x4 ldsrf4(u32 a) { return *(LDS f32x4*)(size_t)a; }
DI void ldsw16(u32 a, u32x4 v) { *(LDS u32x4*)(size_t)a = v; }
DI void ldswf4(u32 a, f32x4 v) { *(LDS f32x4*)(size_t)a = v; }

template <int CTRL, int RM> DI float dppadd(float v) { return v + __builtin_bit_cast(float, __builtin_amdgcn_update_dpp(0, __builtin_bit_cast(int, v), CTRL, RM, 0xf, false)); }
DI float wavesum(float v) {
  v = dppadd<0x111, 0xf>(v); v = dppadd<0x112, 0xf>(v); v = dppadd<0x114, 0xf>(v); v = dppadd<0x118, 0xf>(v);
  v = dppadd<0x142, 0xa>(v); v = dppadd<0x143, 0xc>(v);
  return __builtin_bit_cast(float, __builtin_amdgcn_readlane(__builtin_bit_cast(int, v), 63));
}
DI float xor32sum(float v) { auto p = __builtin_amdgcn_permlane32_swap(asu(v), asu(v), false, false); return asf(p[0]) + asf(p[1]); }
DI float expf_(float t) { return __builtin_amdgcn_exp2f(t * 1.44269504089f); }
DI float phi(float t) { return (t > 0.f ? t + 1.f : expf_(t)) + 1e-6f; }
DI float gelu(float t) { float y = 0.7978845608f * (t + 0.044715f * t * t * t); return t * __builtin_amdgcn_rcpf(1.f + __builtin_amdgcn_exp2f(-2.88539008178f * y)); }

DI u64 mixtag(u64 e) { e ^= e >> 29; e *= 0x9E3779B97F4A7C15ull; e ^= e >> 32; e += 0x632BE59BD9B4E019ull; return e; }

DI void poll2(rsrc_t ws, u32 voffA, u32 voffB, u32 tlo, u32 thi) {
  for (int it = 0; it < 400000; ++it) {
    u32x2 a, b;
    asm volatile("buffer_load_dwordx2 %0, %2, %4, 0 offen sc1\n\tbuffer_load_dwordx2 %1, %3, %4, 0 offen sc1\n\ts_waitcnt vmcnt(0)" : "=&v"(a), "=&v"(b) : "v"(voffA), "v"(voffB), "s"(ws) : "memory");
    bool ok = a.x == tlo && a.y == thi && b.x == tlo && b.y == thi;
    if (__all(ok)) break;
    __builtin_amdgcn_s_sleep(1);
  }
}
DI u32 blk_g1(int ti, int tj) { int id = tj * 8 + ti; return (u32)((id % 28) * 8 + id / 28); }
DI u32 blk_qkv(int T, int Hq) { return (u32)((((T & 7) * 4 + (Hq & 3)) * 8) + (T >> 3) * 2 + (Hq >> 2)); }

#define STAMP(i) do {} while (0)
#define FSTAMP(i) do {} while (0)

struct Ctx {
  rsrc_t rW1, rW2, rWS;
  int lane, hi, kg, dh;
};

DI void reduce_scatter(const f32x16& a0, const f32x16& a1, int lane, int kg, int dh, float (&v)[8]) {
  lbar();
  u32 wb = (u32)(dh * 32768 + kg * 2048 + lane * 16);
#pragma unroll
  for (int p = 0; p < 4; ++p) {
    const f32x16& a = (p >> 1) ? a1 : a0;
    const int o = (p & 1) * 8;
    ldswf4(wb + p * 8192, f32x4{a[o], a[o + 1], a[o + 2], a[o + 3]});
    ldswf4(wb + p * 8192 + 1024, f32x4{a[o + 4], a[o + 5], a[o + 6], a[o + 7]});
  }
  lbar();
  u32 rb = (u32)(dh * 32768 + kg * 8192 + lane * 16);
  f32x4 s0 = ldsrf4(rb), s1 = ldsrf4(rb + 1024);
#pragma unroll
  for (int k = 1; k < 4; ++k) { s0 += ldsrf4(rb + k * 2048); s1 += ldsrf4(rb + k * 2048 + 1024); }
  v[0] = s0[0]; v[1] = s0[1]; v[2] = s0[2]; v[3] = s0[3]; v[4] = s1[0]; v[5] = s1[1]; v[6] = s1[2]; v[7] = s1[3];
}

__global__ __launch_bounds__(512, 2) void fused_kernel(const u16* __restrict__ x, const u16* __restrict__ wqkv, const u16* __restrict__ wo,
                                                       const u16* __restrict__ w1, const u16* __restrict__ b1, const u16* __restrict__ w2,
                                                       const u16* __restrict__ b2, const u16* __restrict__ gw, u16* __restrict__ out, char* __restrict__ wsp) {
  extern __shared__ __attribute__((aligned(16))) char smem[];
  const int tid = threadIdx.x, lane = tid & 63;
  const int w = __builtin_amdgcn_readfirstlane(tid >> 6);
  const int b = blockIdx.x, xcd = b & 7, r = b >> 3;
  const int hi = lane >> 5, l31 = lane & 31;
  const int blk = (lane >> 4) & 1, q = (lane & 15) >> 2, p = lane & 3;
  STAMP(0); FSTAMP(60);

  const rsrc_t rX = mkrsrc(x, S * D * 2), rWQ = mkrsrc(wqkv, D * 1536 * 2), rWS = mkrsrc(wsp, WS_USED);
  const rsrc_t rW1 = mkrsrc(w1, S * F * 2), rW2 = mkrsrc(w2, F * S * 2);

  u32 cdummy = 0;
  if (w == 7) {
    u64 pc; u32 len;
    asm volatile("s_getpc_b64 %0\n.Lgm_begin:\n\ts_mov_b32 %1, .Lgm_end-.Lgm_begin" : "=s"(pc), "=s"(len));
    const char* cp = (const char*)(pc & ~127ull);
    for (u32 o = lane * 128; o < len; o += 8192) asm volatile("global_load_dword %0, %1, off" : "+v"(cdummy) : "v"(cp + o));
  }

  const int T = 8 * (xcd >> 1) + (r >> 2), Hq = 4 * (xcd & 1) + (r & 3);
  u32x2 ev; u32x4 xr, gv;
  ld8c(ev, 0, rWS, OFF_EPOCH);
  {
    const int row = 64 * T + 8 * Hq + w;
    ld16p(xr, lane * 16, rX, row * 1024);
    ld16p(gv, lane * 16, mkrsrc(gw, D * 2), 0);
  }
  const u32 vx = (u32)((64 * T + 8 * w + (lane >> 3)) * 1024 + (((lane & 7) ^ ((4 * w + (lane >> 4)) & 7)) * 16));
  const u32 vw = (u32)((8 * w + (lane >> 3)) * 3072 + Hq * 128 + (((lane & 7) ^ (((lane >> 4) & 1) << 2)) * 16));
  const int qstart = ROT ? ((r >> 2) + 2 * (r & 3)) & 7 : 0;
  auto qkv_issue = [&](int c) {
    const int cc = (c + qstart) & 7;
    const u32 sl = (u32)((c & 3) * 32768 + w * 1024);
    dma16<0>(sl, vx, rX, cc * 128);
    dma16<0>(sl + 8192, vw, rWQ, cc * 64 * 3072);
    dma16<0>(sl + 16384, vw, rWQ, cc * 64 * 3072 + 1024);
    dma16<0>(sl + 24576, vw, rWQ, cc * 64 * 3072 + 2048);
  };
  FSTAMP(8);
  qkv_issue(0); FSTAMP(9); qkv_issue(1); qkv_issue(2); qkv_issue(3);
  FSTAMP(10);
  asm volatile("s_waitcnt vmcnt(16)" : "+v"(ev), "+v"(xr), "+v"(gv), "+v"(cdummy)::"memory");
  FSTAMP(11);
  const u64 tag = mixtag(((u64)(u32)__builtin_amdgcn_readfirstlane(ev.y) << 32) | (u32)__builtin_amdgcn_readfirstlane(ev.x));
  const u32 tlo = (u32)tag, thi = (u32)(tag >> 32);
  const u32x2 tagv = {tlo, thi};
  {
    float v[8] = {blo(xr.x), bhi(xr.x), blo(xr.y), bhi(xr.y), blo(xr.z), bhi(xr.z), blo(xr.w), bhi(xr.w)};
    float gq[8] = {blo(gv.x), bhi(gv.x), blo(gv.y), bhi(gv.y), blo(gv.z), bhi(gv.z), blo(gv.w), bhi(gv.w)};
    float s = 0.f, gs = 0.f;
#pragma unroll
    for (int j = 0; j < 8; ++j) { s += v[j]; gs += v[j] * gq[j]; }
    const float mean = wavesum(s) * (1.f / 512.f);
    gs = wavesum(gs);
    float ss = 0.f;
#pragma unroll
    for (int j = 0; j < 8; ++j) { v[j] -= mean; ss += v[j] * v[j]; }
    const float rstd = __builtin_amdgcn_rsqf(wavesum(ss) * (1.f / 512.f) + 1e-5f);
    u32x4 h = {cvtpk(v[0] * rstd, v[1] * rstd), cvtpk(v[2] * rstd, v[3] * rstd), cvtpk(v[4] * rstd, v[5] * rstd), cvtpk(v[6] * rstd, v[7] * rstd)};
    ldsw16(L_LN + w * 1088 + lane * 16, h);
    FSTAMP(12);
    lbar();
    FSTAMP(13);
    const u32 la = L_LN + (u32)(q * 1088 + ((tid >> 4) * 16 + 4 * p) * 2);
    bf16x8 ht = trfrag(la, la + 4 * 1088);
    st16c(__builtin_bit_cast(u32x4, ht), tid * 16, rWS, OFF_HT + (8 * T + Hq) * 8192);
    const float g = __builtin_amdgcn_rcpf(1.f + expf_(-gs));
    if (lane == 0) st4c(asu(g), 0, rWS, OFF_G + (64 * T + 8 * Hq + w) * 4);
  }
  STAMP(1); FSTAMP(61);
  const int th = w & 1, hp = (w >> 1) & 1;
  f32x16 accq = {}, acck = {}, accv = {};
  u32 ax[4];
  {
    const int row = 32 * th + l31, f = (row >> 1) & 7;
#pragma unroll
    for (int ks = 0; ks < 4; ++ks) ax[ks] = (u32)(row * 128 + (((2 * ks + hi) ^ f) * 16));
  }
  const u32 aw = (u32)(8192 + (8 * hi + q) * 128 + (((4 * hp + 2 * blk + (p >> 1)) ^ (((q >> 1) & 1) << 2)) * 16) + 8 * (p & 1));
  auto qkv_comp = [&](int c) {
    if (w < 4) {
      const u32 sl = (u32)((c & 3) * 32768);
#pragma unroll
      for (int ks = 0; ks < 4; ++ks) {
        const bf16x8 xf = frag(ldsr16(sl + ax[ks]));
        const u32 a = sl + aw + ks * 2048;
        const bf16x8 fq = trfrag(a, a + 512), fk = trfrag(a + 8192, a + 8192 + 512), fv = trfrag(a + 16384, a + 16384 + 512);
        accq = mfma(fq, xf, accq);
        acck = mfma(xf, fk, acck);
        accv = mfma(xf, fv, accv);
      }
    }
  };
  WAIT_BAR(14); FSTAMP(0); qkv_comp(0);
  WAIT_BAR(10); FSTAMP(1); qkv_issue(4); qkv_comp(1);
  WAIT_BAR(10); FSTAMP(2); qkv_issue(5); qkv_comp(2);
  WAIT_BAR(10); FSTAMP(3); qkv_issue(6); qkv_comp(3);
  WAIT_BAR(8); FSTAMP(4); qkv_issue(7);
  if (tid == 0) st8c(tagv, 0, rWS, OFF_FA + b * 64);
  qkv_comp(4);
  WAIT_BAR(8); FSTAMP(5); qkv_comp(5);
  WAIT_BAR(4); FSTAMP(6); qkv_comp(6);
  WAIT_BAR(0); FSTAMP(7);
  if (w == 4) poll2(rWS, OFF_FA + lane * 256, OFF_FA + lane * 256 + 64, tlo, thi), poll2(rWS, OFF_FA + lane * 256 + 128, OFF_FA + lane * 256 + 192, tlo, thi);
  qkv_comp(7);
  STAMP(2); FSTAMP(14);
  u32 pq[8];
  {
    f32x16 kvacc = {};
    float zs = 0.f;
    if (w < 4) {
      u32 pk[8], vv[8];
#pragma unroll
      for (int i = 0; i < 8; ++i) {
        const u32 kb = cvtpk(acck[2 * i], acck[2 * i + 1]);
        pk[i] = cvtpk(phi(blo(kb)), phi(bhi(kb)));
        zs += blo(pk[i]) + bhi(pk[i]);
        vv[i] = cvtpk(accv[2 * i], accv[2 * i + 1]);
        const u32 qb = cvtpk(accq[2 * i], accq[2 * i + 1]);
        pq[i] = cvtpk(phi(blo(qb)), phi(bhi(qb)));
      }
      kvacc = mfma(frag(u32x4{pk[0], pk[1], pk[2], pk[3]}), frag(u32x4{vv[0], vv[1], vv[2], vv[3]}), kvacc);
      kvacc = mfma(frag(u32x4{pk[4], pk[5], pk[6], pk[7]}), frag(u32x4{vv[4], vv[5], vv[6], vv[7]}), kvacc);
      zs = xor32sum(zs);
    } else {
#pragma unroll
      for (int i = 0; i < 8; ++i) pq[i] = 0;
    }
    f32x4 k0, k1;
#pragma unroll
    for (int i = 0; i < 4; ++i) { k0[i] = blk ? kvacc[8 + i] : kvacc[i]; k1[i] = blk ? kvacc[12 + i] : kvacc[4 + i]; }
    FSTAMP(15);
    const u32 sc = L_KVR + hp * 2304;
    if (w < 4 && th == 1) {
      ldswf4(sc + lane * 32, k0); ldswf4(sc + lane * 32 + 16, k1);
      if (lane < 32) *(LDS float*)(size_t)(sc + 2048 + lane * 4) = zs;
    }
    lbar();
    FSTAMP(50);
    if (w < 4 && th == 0) {
      k0 += ldsrf4(sc + lane * 32); k1 += ldsrf4(sc + lane * 32 + 16);
      zs += *(LDS float*)(size_t)(sc + 2048 + l31 * 4);
      const int head = 4 * Hq + 2 * hp + blk;
      const u32 vo = (u32)(head * 32768 + (hi * 16 + (lane & 15)) * 32);
      st16c(__builtin_bit_cast(u32x4, k0), vo, rWS, OFF_KVP + T * 1024);
      st16c(__builtin_bit_cast(u32x4, k1), vo + 16, rWS, OFF_KVP + T * 1024);
      if (lane < 32) st4c(asu(zs), lane * 4, rWS, OFF_ZP + (Hq * 32 + T) * 256 + hp * 128);
    }
  }
  STAMP(3); FSTAMP(51);

  const int kg = w & 3, dh = w >> 2;
  const int st = xcd * 4 + (r >> 3), dt = r & 7;
  u32 gS0, gS1, b2S0, b2S1, gT;
  ld4c(gS0, (64 * st + l31) * 4, rWS, OFF_G);
  ld4c(gS1, (64 * st + 32 + l31) * 4, rWS, OFF_G);
  ld4c(gT, (64 * T + 32 * th + l31) * 4, rWS, OFF_G);
  {
    const rsrc_t rB2 = mkrsrc(b2, S * 2);
    ld2p(b2S0, (64 * st + l31) * 2, rB2, 0);
    ld2p(b2S1, (64 * st + 32 + l31) * 2, rB2, 0);
  }
  const u32 swz = (u32)(((lane & 7) ^ (((lane >> 4) & 1) << 2)) * 16);
  const u32 vW2 = (u32)((8 * dh + (lane >> 3)) * 4096 + st * 128) + swz;
  const int sC = ROT ? (((r & 7) * 14) >> 3) : 0;
  auto w2_issue = [&](int j) {
    int jj = j + sC; if (jj >= 14) jj -= 14;
    const u32 sl = (u32)(((j + 4) % 9) * 16384 + kg * 4096 + dh * 1024);
    const u32 so = (u32)((448 * kg + 32 * jj) * 4096);
    dma16<AUX_W2>(sl, vW2, rW2, so);
    dma16<AUX_W2>(sl + 2048, vW2, rW2, so + 16 * 4096);
  };
  u32x2 zg;
  if (b == 0 && tid == 0) st8c(tagv, 0, rWS, OFF_EPOCH);

  if (r < 28) {
    const int id = xcd * 28 + r, ti = id & 7, tj = id >> 3;
    u32x2 b1a, b1b;
    {
      const rsrc_t rB1 = mkrsrc(b1, F * 2);
      const u32 f0 = (u32)(64 * tj + 32 * (kg >> 1) + 16 * (kg & 1) + 4 * hi) * 2;
      ld8p(b1a, f0, rB1, 0); ld8p(b1b, f0 + 16, rB1, 0);
    }
    const u32 vW1 = (u32)((8 * dh + (lane >> 3)) * 3584 + tj * 128) + swz;
    const u32 vH = (u32)(hi * 8192 + (64 * ti + 32 * dh + l31) * 16);
    const int sB = ROT ? ((2 * ti + 4 * (tj & 3) + ((tj >> 2) & 1)) & 15) : 0;
    u32x4 hr[4][2];
    auto g1_issue = [&](int s, u32x4 (&h)[2]) {
      const int ss = (s + sB) & 15;
      const u32 sl = (u32)((s & 3) * 16384 + kg * 4096 + dh * 1024);
      const u32 so = (u32)((512 * kg + 32 * ss) * 3584);
      dma16<AUX_W1>(sl, vW1, rW1, so);
      dma16<AUX_W1>(sl + 2048, vW1, rW1, so + 16 * 3584);
      const u32 sh = (u32)(OFF_HT + (32 * kg + 2 * ss) * 16384);
      ld16c(h[0], vH, rWS, sh);
      ld16c(h[1], vH, rWS, sh + 16384);
    };
    const u32 aA0 = (u32)(kg * 4096 + (8 * hi + q) * 128 + (((2 * blk + (p >> 1)) ^ (((q >> 1) & 1) << 2)) * 16) + 8 * (p & 1));
    const u32 aA1 = aA0 ^ 64u;
    f32x16 a0 = {}, a1 = {};
    FSTAMP(52);
    g1_issue(0, hr[0]); FSTAMP(53); g1_issue(1, hr[1]); g1_issue(2, hr[2]);
    STAMP(4); FSTAMP(54);
#define G1_STAGE(SI)                                                                                              \
  {                                                                                                               \
    asm volatile("s_waitcnt vmcnt(8) lgkmcnt(0)\n\ts_barrier" : "+v"(hr[(SI) & 3][0]), "+v"(hr[(SI) & 3][1])::"memory"); \
    FSTAMP(16 + (SI));                                                                                            \
    if ((SI) == 0 && tid == 0) st8c(tagv, 0, rWS, OFF_FQ + b * 64);                                               \
    if ((SI) + 3 < 16) g1_issue((SI) + 3, hr[((SI) + 3) & 3]);                                                    \
    else if ((SI) < 15) { w2_issue(2 * ((SI) - 13)); w2_issue(2 * ((SI) - 13) + 1); }                             \
    else w2_issue(4);                                                                                             \
    if ((SI) == 12) ld8c(zg, l31 * 8, rWS, OFF_ZF + (4 * Hq + 2 * hp) * 128);                                     \
    _Pragma("unroll") for (int ks = 0; ks < 2; ++ks) {                                                            \
      const u32 o = (u32)(((SI) & 3) * 16384 + ks * 2048);                                                        \
      const bf16x8 f0 = trfrag(aA0 + o, aA0 + o + 512), f1 = trfrag(aA1 + o, aA1 + o + 512);                      \
      a0 = mfma(f0, frag(hr[(SI) & 3][ks]), a0);                                                                  \
      a1 = mfma(f1, frag(hr[(SI) & 3][ks]), a1);                                                                  \
    }                                                                                                             \
  }
    G1_STAGE(0) G1_STAGE(1) G1_STAGE(2) G1_STAGE(3) G1_STAGE(4) G1_STAGE(5) G1_STAGE(6) G1_STAGE(7)
    G1_STAGE(8) G1_STAGE(9) G1_STAGE(10) G1_STAGE(11) G1_STAGE(12) G1_STAGE(13) G1_STAGE(14) G1_STAGE(15)
    STAMP(5); FSTAMP(55);
    float v[8];
    reduce_scatter(a0, a1, lane, kg, dh, v);
    FSTAMP(56);
    asm volatile("" : "+v"(b1a), "+v"(b1b));
    const float bb[8] = {blo(b1a.x), bhi(b1a.x), blo(b1a.y), bhi(b1a.y), blo(b1b.x), bhi(b1b.x), blo(b1b.y), bhi(b1b.y)};
    u32x4 uo;
    uo.x = cvtpk(gelu(v[0] + bb[0]), gelu(v[1] + bb[1])); uo.y = cvtpk(gelu(v[2] + bb[2]), gelu(v[3] + bb[3]));
    uo.z = cvtpk(gelu(v[4] + bb[4]), gelu(v[5] + bb[5])); uo.w = cvtpk(gelu(v[6] + bb[6]), gelu(v[7] + bb[7]));
    st16c(uo, vH, rWS, OFF_UT + (4 * tj + kg) * 16384);
  } else {
    const int h = xcd * 4 + (r - 28);
    const rsrc_t rWO = mkrsrc(wo, D * D * 2);
    dma16<0>((u32)(2 * w * 1024), lane * 16, rWO, (16 * h + 2 * w) * 1024);
    dma16<0>((u32)((2 * w + 1) * 1024), lane * 16, rWO, (16 * h + 2 * w + 1) * 1024);
    w2_issue(0); w2_issue(1); w2_issue(2); w2_issue(3); w2_issue(4);
    waitvm<10>();
    lbar();
    if (tid == 0) st8c(tagv, 0, rWS, OFF_FQ + b * 64);
    if (w == 0) { const u32 fo = OFF_FQ + blk_qkv(l31, h >> 2) * 64; poll2(rWS, fo, fo, tlo, thi); }
    lbar();
    u32x4 k4[4];
#pragma unroll
    for (int i = 0; i < 4; ++i) ld16c(k4[i], lane * 16, rWS, OFF_KVP + h * 32768 + (4 * w + i) * 1024);
    u32x4 z4[2];
#pragma unroll
    for (int i = 0; i < 2; ++i) ld16c(z4[i], ((lane >> 2) + 16 * i) * 256 + (lane & 3) * 16, rWS, OFF_ZP + (h >> 2) * 8192 + (h & 3) * 64);
    asm volatile("s_waitcnt vmcnt(0)" : "+v"(k4[0]), "+v"(k4[1]), "+v"(k4[2]), "+v"(k4[3]), "+v"(z4[0]), "+v"(z4[1])::"memory");
    f32x4 ks = __builtin_bit_cast(f32x4, k4[0]) + __builtin_bit_cast(f32x4, k4[1]) + __builtin_bit_cast(f32x4, k4[2]) + __builtin_bit_cast(f32x4, k4[3]);
    ldswf4(16384 + w * 1024 + lane * 16, ks);
    if (w == 0) {
      f32x4 zz = __builtin_bit_cast(f32x4, z4[0]) + __builtin_bit_cast(f32x4, z4[1]);
#pragma unroll
      for (int i = 0; i < 4; ++i) {
        float t = zz[i];
        t += __shfl_xor(t, 4); t += __shfl_xor(t, 8); t += __shfl_xor(t, 16); t += __shfl_xor(t, 32);
        zz[i] = t;
      }
      if (lane < 4) ldswf4(25600 + lane * 16, zz);
    }
    lbar();
    if (w == 0) {
      f32x4 t = ldsrf4(16384 + lane * 16);
#pragma unroll
      for (int i = 1; i < 8; ++i) t += ldsrf4(16384 + i * 1024 + lane * 16);
      const int half = lane & 1, e = (lane >> 1) & 15, hl = lane >> 5;
      const u32 pk2 = cvtpk(t[0], t[1]), pk3 = cvtpk(t[2], t[3]);
      const u32 o = 24576 + (8 * half + 4 * hl) * 32 + e * 2;
      *(LDS u16*)(size_t)(o) = (u16)pk2; *(LDS u16*)(size_t)(o + 32) = (u16)(pk2 >> 16);
      *(LDS u16*)(size_t)(o + 64) = (u16)pk3; *(LDS u16*)(size_t)(o + 96) = (u16)(pk3 >> 16);
      if (lane < 16) { const float z = *(LDS float*)(size_t)(25600 + lane * 4); st8c(u32x2{asu(z), tlo}, lane * 8, rWS, OFF_ZF + h * 128); }
    }
    lbar();
    u32x4 kf = ldsr16(24576 + (l31 & 15) * 32 + hi * 16);
    if (l31 >= 16) kf = u32x4{0, 0, 0, 0};
#pragma unroll
    for (int i = 0; i < 2; ++i) {
      const int dtile = 2 * w + i;
      const u32 a = (u32)((8 * hi + q) * 1024 + dtile * 64 + (2 * blk + (p >> 1)) * 16 + 8 * (p & 1));
      const bf16x8 fo = trfrag(a, a + 4096);
      f32x16 m = {};
      m = mfma(frag(kf), fo, m);
      st16c(u32x4{cvtpk(m[0], m[1]), cvtpk(m[2], m[3]), cvtpk(m[4], m[5]), cvtpk(m[6], m[7])}, lane * 16, rWS, OFF_M + (h * 16 + dtile) * 1024);
    }
    waitvm<0>();
    lbar();
    if (tid == 0) st8c(tagv, 0, rWS, OFF_FM + h * 64);
    ld8c(zg, l31 * 8, rWS, OFF_ZF + (4 * Hq + 2 * hp) * 128);
  }
  asm volatile("s_waitcnt vmcnt(0)" : "+v"(zg), "+v"(gT), "+v"(gS0), "+v"(gS1), "+v"(b2S0), "+v"(b2S1)::"memory");
  if (w < 4) {
    for (int it = 0; it < 400000; ++it) {
      if (__all(zg.y == tlo)) break;
      __builtin_amdgcn_s_sleep(1);
      asm volatile("buffer_load_dwordx2 %0, %1, %2, %3 offen sc1\n\ts_waitcnt vmcnt(0)" : "=&v"(zg) : "v"(l31 * 8), "s"(rWS), "s"((u32)(OFF_ZF + (4 * Hq + 2 * hp) * 128)) : "memory");
    }
    const u32 zs = L_MISC + w * 128;
    if (lane < 32) *(LDS u32*)(size_t)(zs + lane * 4) = zg.x;
    asm volatile("s_waitcnt lgkmcnt(0)" ::: "memory");
#pragma unroll
    for (int par = 0; par < 2; ++par) {
      const f32x4 z0 = ldsrf4(zs + par * 64 + hi * 16), z1 = ldsrf4(zs + par * 64 + 32 + hi * 16);
      float e[8];
#pragma unroll
      for (int i = 0; i < 4; ++i) { e[2 * i] = blo(pq[4 * par + i]); e[2 * i + 1] = bhi(pq[4 * par + i]); }
      float den = e[0] * z0[0] + e[1] * z0[1] + e[2] * z0[2] + e[3] * z0[3] + e[4] * z1[0] + e[5] * z1[1] + e[6] * z1[2] + e[7] * z1[3];
      den = xor32sum(den) + 1e-6f;
      const float sc = asf(gT) * __builtin_amdgcn_rcpf(den);
      st16c(u32x4{cvtpk(e[0] * sc, e[1] * sc), cvtpk(e[2] * sc, e[3] * sc), cvtpk(e[4] * sc, e[5] * sc), cvtpk(e[6] * sc, e[7] * sc)}, lane * 16, rWS,
            OFF_PQ + ((4 * Hq + 2 * hp + par) * 64 + 2 * T + th) * 1024);
    }
  }
  FSTAMP(57);
  waitvm<0>();
  FSTAMP(58);
  lbar();
  if (tid == 0) st8c(tagv, 0, rWS, OFF_FU + b * 64);
  STAMP(6); FSTAMP(59);

  w2_issue(5); w2_issue(6); w2_issue(7); w2_issue(8);
  if (w == 0) {
    const u32 fa = OFF_FU + (lane < 28 ? blk_g1(dt, lane) : blk_qkv(st, lane & 7)) * 64;
    poll2(rWS, fa, OFF_FM + l31 * 64, tlo, thi);
  }
  lbar();
  STAMP(7);
  {
    const u32 vU = (u32)(hi * 8192 + (64 * dt + 32 * dh + l31) * 16);
    const u32 aB0 = (u32)(kg * 4096 + (4 * hi + q) * 128 + (((2 * blk + (p >> 1)) ^ (((q >> 1) & 1) << 2)) * 16) + 8 * (p & 1));
    const u32 aB1 = aB0 ^ 64u;
    u32x4 ur[6][2];
    auto dir_issue = [&](int j, u32x4 (&u)[2]) {
      if (j < 14) {
        int jj = j + sC; if (jj >= 14) jj -= 14;
        const u32 so = (u32)(OFF_UT + (28 * kg + 2 * jj) * 16384);
        ld16c(u[0], vU, rWS, so); ld16c(u[1], vU, rWS, so + 16384);
      } else {
        const u32 so = (u32)(OFF_M + ((8 * kg + 2 * (j - 14)) * 16 + 2 * dt + dh) * 1024);
        ld16c(u[0], lane * 16, rWS, so); ld16c(u[1], lane * 16, rWS, so + 16384);
      }
    };
    auto lds_issue = [&](int j) {
      if (j < 14) w2_issue(j);
      else {
        const u32 sl = (u32)(((j + 4) % 9) * 16384 + kg * 4096 + dh * 1024);
        const u32 so = (u32)(OFF_PQ + ((8 * kg + 2 * (j - 14)) * 64 + 2 * st + dh) * 1024);
        dma16<16>(sl, lane * 16, rWS, so); dma16<16>(sl + 2048, lane * 16, rWS, so + 65536);
      }
    };
    f32x16 a0 = {}, a1 = {};
    dir_issue(0, ur[0]); dir_issue(1, ur[1]); dir_issue(2, ur[2]); dir_issue(3, ur[3]); dir_issue(4, ur[4]);
#define C_OPS(i) (((i) >= 1 && (i) <= 9 ? 2 : 0) + ((i) >= 0 && (i) <= 12 ? 2 : 0))
#define C_MIN(a, b) ((a) < (b) ? (a) : (b))
#define C_ND(J) (((J) < 5 ? 2 * (4 - (J)) : 0) + C_OPS((J)-4) + C_OPS((J)-3) + C_OPS((J)-2) + C_OPS((J)-1))
#define C_NL(J) ((J) < 9 ? 63 : 2 + C_OPS((J)-7) + C_OPS((J)-6) + C_OPS((J)-5) + C_OPS((J)-4) + C_OPS((J)-3) + C_OPS((J)-2) + C_OPS((J)-1))
#define C_STAGE(J)                                                                                                \
  {                                                                                                               \
    asm volatile("s_waitcnt vmcnt(%2) lgkmcnt(0)\n\ts_barrier" : "+v"(ur[(J) % 6][0]), "+v"(ur[(J) % 6][1]) : "i"(C_MIN(C_ND(J), C_NL(J))) : "memory"); \
    FSTAMP(32 + (J));                                                                                             \
    if ((J) >= 1 && (J) <= 9) lds_issue((J) + 8);                                                                 \
    if ((J) <= 12) dir_issue((J) + 5, ur[((J) + 5) % 6]);                                                         \
    if ((J) == 14) { const float s0 = 1.f - asf(gS0), s1 = 1.f - asf(gS1); a0 *= s0; a1 *= s1; }                  \
    _Pragma("unroll") for (int ks = 0; ks < 2; ++ks) {                                                            \
      const u32 o = (u32)((((J) + 4) % 9) * 16384 + ks * 2048);                                                   \
      bf16x8 f0, f1;                                                                                              \
      if ((J) < 14) { f0 = trfrag(aB0 + o, aB0 + o + 1024); f1 = trfrag(aB1 + o, aB1 + o + 1024); }               \
      else { f0 = frag(ldsr16(o + kg * 4096 + lane * 16)); f1 = frag(ldsr16(o + kg * 4096 + 1024 + lane * 16)); } \
      a0 = mfma(frag(ur[(J) % 6][ks]), f0, a0);                                                                   \
      a1 = mfma(frag(ur[(J) % 6][ks]), f1, a1);                                                                   \
    }                                                                                                             \
  }
    C_STAGE(0) C_STAGE(1) C_STAGE(2) C_STAGE(3) C_STAGE(4) C_STAGE(5) C_STAGE(6) C_STAGE(7) C_STAGE(8)
    C_STAGE(9) C_STAGE(10) C_STAGE(11) C_STAGE(12) C_STAGE(13) C_STAGE(14) C_STAGE(15) C_STAGE(16) C_STAGE(17)
    STAMP(8);
    float v[8];
    reduce_scatter(a0, a1, lane, kg, dh, v);
    const float gs = (kg >> 1) ? asf(gS1) : asf(gS0);
    const float bias = (1.f - gs) * blo((kg >> 1) ? b2S1 : b2S0);
    u32 a_x = cvtpk(v[0] + bias, v[1] + bias), a_y = cvtpk(v[2] + bias, v[3] + bias), b_x = cvtpk(v[4] + bias, v[5] + bias), b_y = cvtpk(v[6] + bias, v[7] + bias);
    auto s0 = __builtin_amdgcn_permlane32_swap(a_x, b_x, false, false);
    auto s1 = __builtin_amdgcn_permlane32_swap(a_y, b_y, false, false);
    const rsrc_t rO = mkrsrc(out, S * D * 2);
    st16p(u32x4{s0[0], s1[0], s0[1], s1[1]}, (u32)((64 * st + 32 * (kg >> 1) + l31) * 1024 + (64 * dt + 32 * dh + 16 * (kg & 1) + 8 * hi) * 2), rO, 0);
  }
  STAMP(9); FSTAMP(62);
  asm volatile(".Lgm_end:" ::: "memory");
}

extern "C" void kernel_launch(void* const* d_in, const int* in_sizes, int n_in, void* d_out, int out_size, void* d_ws, size_t ws_size, hipStream_t stream) {
  (void)in_sizes; (void)n_in; (void)out_size; (void)ws_size;
  (void)hipFuncSetAttribute((const void*)fused_kernel, hipFuncAttributeMaxDynamicSharedMemorySize, LDS_BYTES);
  fused_kernel<<<256, 512, LDS_BYTES, stream>>>((const u16*)d_in[0], (const u16*)d_in[1], (const u16*)d_in[2], (const u16*)d_in[4], (const u16*)d_in[5],
                                               (const u16*)d_in[6], (const u16*)d_in[7], (const u16*)d_in[8], (u16*)d_out, (char*)d_ws);
}
```

```cpp
#include <hip/hip_runtime.h>
#include <stdint.h>
#include <type_traits>

typedef unsigned short u16;
typedef unsigned int u32;
typedef unsigned long long u64;
typedef __bf16 bf16x8 __attribute__((ext_vector_type(8)));
typedef __bf16 bf16x2 __attribute__((ext_vector_type(2)));
typedef short s16x4 __attribute__((ext_vector_type(4)));
typedef short s16x8 __attribute__((ext_vector_type(8)));
typedef float f32x2 __attribute__((ext_vector_type(2)));
typedef float f32x4 __attribute__((ext_vector_type(4)));
typedef float f32x16 __attribute__((ext_vector_type(16)));
typedef u32 u32x2 __attribute__((ext_vector_type(2)));
typedef u32 u32x4 __attribute__((ext_vector_type(4)));
typedef __amdgpu_buffer_rsrc_t rsrc_t;
#define DI __device__ __forceinline__
#define LDS __attribute__((address_space(3)))

#define STAMPS 0
#define AUX_W1 0
#define AUX_W2 0
#define ROT 1
#define ORD1 1
#define ORD2 1

constexpr int S = 2048, D = 512, F = 1792;

constexpr u32 OFF_EPOCH = 0;
constexpr u32 OFF_FA = 4096;
constexpr u32 OFF_FQ = OFF_FA + 16384;
constexpr u32 OFF_FU = OFF_FQ + 16384;
constexpr u32 OFF_FM = OFF_FU + 16384;
constexpr u32 OFF_ZF = OFF_FM + 4096;
constexpr u32 OFF_G = 65536;
constexpr u32 OFF_ZP = OFF_G + 8192;
constexpr u32 OFF_KVP = 262144;
constexpr u32 OFF_HT = OFF_KVP + 1048576;
constexpr u32 OFF_PQ = OFF_HT + 2097152;
constexpr u32 OFF_M = OFF_PQ + 2097152;
constexpr u32 OFF_UT = OFF_M + 524288;
constexpr u32 OFF_TS = OFF_UT + 1835008;
constexpr u32 WS_USED = OFF_TS + 4 * 65536;

constexpr u32 L_LN = 131072;
constexpr u32 L_KVR = 147456;
constexpr u32 L_PQX = L_KVR + 4608;
constexpr u32 L_SYNC = 163840 - 64;
constexpr u32 LDS_BYTES = 163840;

DI rsrc_t mkrsrc(const void* p, u32 bytes) { return __builtin_amdgcn_make_buffer_rsrc((void*)p, (short)0, (int)bytes, 0x00020000); }
DI u32 cvtpk(float a, float b) { f32x2 v = {a, b}; bf16x2 r = __builtin_convertvector(v, bf16x2); return __builtin_bit_cast(u32, r); }
DI float blo(u32 v) { return __builtin_bit_cast(float, v << 16); }
DI float bhi(u32 v) { return __builtin_bit_cast(float, v & 0xffff0000u); }
DI float asf(u32 v) { return __builtin_bit_cast(float, v); }
DI u32 asu(float v) { return __builtin_bit_cast(u32, v); }
DI bf16x8 frag(u32x4 v) { return __builtin_bit_cast(bf16x8, v); }
DI f32x16 mfma(bf16x8 a, bf16x8 b, f32x16 c) { return __builtin_amdgcn_mfma_f32_32x32x16_bf16(a, b, c, 0, 0, 0); }

template <int AUX> DI void dma16(u32 m0v, u32 voff, rsrc_t r, u32 soff) {
  if constexpr (AUX == 0) asm volatile("s_mov_b32 m0, %0\n\ts_nop 0\n\tbuffer_load_dwordx4 %1, %2, %3 offen lds" ::"s"(m0v), "v"(voff), "s"(r), "s"(soff) : "memory");
  else if constexpr (AUX == 2) asm volatile("s_mov_b32 m0, %0\n\ts_nop 0\n\tbuffer_load_dwordx4 %1, %2, %3 offen nt lds" ::"s"(m0v), "v"(voff), "s"(r), "s"(soff) : "memory");
  else asm volatile("s_mov_b32 m0, %0\n\ts_nop 0\n\tbuffer_load_dwordx4 %1, %2, %3 offen sc1 lds" ::"s"(m0v), "v"(voff), "s"(r), "s"(soff) : "memory");
}
DI void ld16c(u32x4& d, u32 voff, rsrc_t r, u32 soff) { asm volatile("buffer_load_dwordx4 %0, %1, %2, %3 offen sc1" : "=v"(d) : "v"(voff), "s"(r), "s"(soff)); }
DI void ld16p(u32x4& d, u32 voff, rsrc_t r, u32 soff) { asm volatile("buffer_load_dwordx4 %0, %1, %2, %3 offen" : "=v"(d) : "v"(voff), "s"(r), "s"(soff)); }
DI void ld16c_rw(u32x4& d, u32 voff, rsrc_t r, u32 soff) { asm volatile("buffer_load_dwordx4 %0, %1, %2, %3 offen sc1" : "+v"(d) : "v"(voff), "s"(r), "s"(soff)); }
DI void ld8c(u32x2& d, u32 voff, rsrc_t r, u32 soff) { asm volatile("buffer_load_dwordx2 %0, %1, %2, %3 offen sc1" : "=v"(d) : "v"(voff), "s"(r), "s"(soff)); }
DI void ld8p(u32x2& d, u32 voff, rsrc_t r, u32 soff) { asm volatile("buffer_load_dwordx2 %0, %1, %2, %3 offen" : "=v"(d) : "v"(voff), "s"(r), "s"(soff)); }
DI void ld4c(u32& d, u32 voff, rsrc_t r, u32 soff) { asm volatile("buffer_load_dword %0, %1, %2, %3 offen sc1" : "=v"(d) : "v"(voff), "s"(r), "s"(soff)); }
DI void ld2p(u32& d, u32 voff, rsrc_t r, u32 soff) { asm volatile("buffer_load_ushort %0, %1, %2, %3 offen" : "=v"(d) : "v"(voff), "s"(r), "s"(soff)); }
DI void st16c(u32x4 v, u32 voff, rsrc_t r, u32 soff) { asm volatile("buffer_store_dwordx4 %0, %1, %2, %3 offen sc1\n\ts_nop 1" ::"v"(v), "v"(voff), "s"(r), "s"(soff) : "memory"); }
DI void st16p(u32x4 v, u32 voff, rsrc_t r, u32 soff) { asm volatile("buffer_store_dwordx4 %0, %1, %2, %3 offen\n\ts_nop 1" ::"v"(v), "v"(voff), "s"(r), "s"(soff) : "memory"); }
DI void st8c(u32x2 v, u32 voff, rsrc_t r, u32 soff) { asm volatile("buffer_store_dwordx2 %0, %1, %2, %3 offen sc1" ::"v"(v), "v"(voff), "s"(r), "s"(soff) : "memory"); }
DI void st4c(u32 v, u32 voff, rsrc_t r, u32 soff) { asm volatile("buffer_store_dword %0, %1, %2, %3 offen sc1" ::"v"(v), "v"(voff), "s"(r), "s"(soff) : "memory"); }
template <int N> DI void waitvm() { asm volatile("s_waitcnt vmcnt(%0)" ::"i"(N) : "memory"); }
#define WAIT_BAR(N) asm volatile("s_waitcnt vmcnt(%0) lgkmcnt(0)\n\ts_barrier" ::"i"(N) : "memory")
DI void SB() { __builtin_amdgcn_sched_barrier(0); }
DI void lbar() { asm volatile("s_waitcnt lgkmcnt(0)\n\ts_barrier" ::: "memory"); }

DI s16x4 trread(u32 a) { return __builtin_amdgcn_ds_read_tr16_b64_v4i16((LDS s16x4*)(size_t)a); }
DI bf16x8 trfrag(u32 a0, u32 a1) { s16x4 lo = trread(a0), hi = trread(a1); return __builtin_bit_cast(bf16x8, (s16x8)__builtin_shufflevector(lo, hi, 0, 1, 2, 3, 4, 5, 6, 7)); }
DI u32x4 ldsr16(u32 a) { return *(LDS u32x4*)(size_t)a; }
DI f32x4 ldsrf4(u32 a) { return *(LDS f32x4*)(size_t)a; }
DI void ldsw16(u32 a, u32x4 v) { *(LDS u32x4*)(size_t)a = v; }
DI void ldswf4(u32 a, f32x4 v) { *(LDS f32x4*)(size_t)a = v; }

template <int CTRL, int RM> DI float dppadd(float v) { return v + __builtin_bit_cast(float, __builtin_amdgcn_update_dpp(0, __builtin_bit_cast(int, v), CTRL, RM, 0xf, false)); }
DI float wavesum(float v) {
  v = dppadd<0x111, 0xf>(v); v = dppadd<0x112, 0xf>(v); v = dppadd<0x114, 0xf>(v); v = dppadd<0x118, 0xf>(v);
  v = dppadd<0x142, 0xa>(v); v = dppadd<0x143, 0xc>(v);
  return __builtin_bit_cast(float, __builtin_amdgcn_readlane(__builtin_bit_cast(int, v), 63));
}
DI void wavesum2(float& a, float& b) {
  a = dppadd<0x111, 0xf>(a); b = dppadd<0x111, 0xf>(b); a = dppadd<0x112, 0xf>(a); b = dppadd<0x112, 0xf>(b);
  a = dppadd<0x114, 0xf>(a); b = dppadd<0x114, 0xf>(b); a = dppadd<0x118, 0xf>(a); b = dppadd<0x118, 0xf>(b);
  a = dppadd<0x142, 0xa>(a); b = dppadd<0x142, 0xa>(b); a = dppadd<0x143, 0xc>(a); b = dppadd<0x143, 0xc>(b);
  a = __builtin_bit_cast(float, __builtin_amdgcn_readlane(__builtin_bit_cast(int, a), 63));
  b = __builtin_bit_cast(float, __builtin_amdgcn_readlane(__builtin_bit_cast(int, b), 63));
}
DI float xor32sum(float v) { auto p = __builtin_amdgcn_permlane32_swap(asu(v), asu(v), false, false); return asf(p[0]) + asf(p[1]); }
DI float expf_(float t) { return __builtin_amdgcn_exp2f(t * 1.44269504089f); }
DI float phi(float t) { return __builtin_fmaxf(t + 1.f, __builtin_amdgcn_exp2f(__builtin_fminf(t * 1.44269504089f, 0.f))) + 1e-6f; }
DI float opaque(float v) { asm volatile("" : "+v"(v)); return v; }
DI float gelu(float t) { float y = 0.7978845608f * (t + 0.044715f * t * t * t); return t * __builtin_amdgcn_rcpf(1.f + __builtin_amdgcn_exp2f(-2.88539008178f * y)); }

DI u64 mixtag(u64 e) { e ^= e >> 29; e *= 0x9E3779B97F4A7C15ull; e ^= e >> 32; e += 0x632BE59BD9B4E019ull; return e; }

DI void poll2(rsrc_t ws, u32 voffA, u32 voffB, u32 tlo, u32 thi) {
  for (int it = 0; it < 400000; ++it) {
    u32x2 a, b;
    asm volatile("buffer_load_dwordx2 %0, %2, %4, 0 offen sc1\n\tbuffer_load_dwordx2 %1, %3, %4, 0 offen sc1\n\ts_waitcnt vmcnt(0)" : "=&v"(a), "=&v"(b) : "v"(voffA), "v"(voffB), "s"(ws) : "memory");
    bool ok = a.x == tlo && a.y == thi && b.x == tlo && b.y == thi;
    if (__all(ok)) break;
    __builtin_amdgcn_s_sleep(1);
  }
}
DI u32 blk_g1(int ti, int tj) { int id = tj * 8 + ti; return (u32)((id % 28) * 8 + id / 28); }
DI u32 blk_qkv(int T, int Hq) { return (u32)((((T & 7) * 4 + (Hq & 3)) * 8) + (T >> 3) * 2 + (Hq >> 2)); }

#define STAMP(i) do {} while (0)
#define FSTAMP(i) do {} while (0)

struct Ctx {
  rsrc_t rW1, rW2, rWS;
  int lane, hi, kg, dh;
};

template <int KG> DI void rs_write(const f32x16& a0, const f32x16& a1, u32 wb, float (&v)[8]) {
#pragma unroll
  for (int p = 0; p < 4; ++p) {
    const f32x16& a = (p >> 1) ? a1 : a0;
    const int o = (p & 1) * 8;
    if (p == KG) {
#pragma unroll
      for (int i = 0; i < 8; ++i) v[i] = a[o + i];
    } else {
      const u32 d = wb + (u32)(p * 6144 + ((KG - p - 1) & 3) * 2048);
      ldswf4(d, f32x4{a[o], a[o + 1], a[o + 2], a[o + 3]});
      ldswf4(d + 1024, f32x4{a[o + 4], a[o + 5], a[o + 6], a[o + 7]});
    }
  }
}
DI void rs_put(const f32x16& a0, const f32x16& a1, u32 base, int lane, int kg, int dh, float (&v)[8]) {
  const u32 wb = base + (u32)(dh * 24576 + lane * 16);
  if (kg == 0) rs_write<0>(a0, a1, wb, v); else if (kg == 1) rs_write<1>(a0, a1, wb, v); else if (kg == 2) rs_write<2>(a0, a1, wb, v); else rs_write<3>(a0, a1, wb, v);
}
DI void rs_get(u32 base, int lane, int kg, int dh, float (&v)[8]) {
  const u32 rb = base + (u32)(dh * 24576 + kg * 6144 + lane * 16);
  f32x4 s0[3], s1[3];
#pragma unroll
  for (int k = 0; k < 3; ++k) { s0[k] = ldsrf4(rb + k * 2048); s1[k] = ldsrf4(rb + k * 2048 + 1024); }
#pragma unroll
  for (int i = 0; i < 4; ++i) { v[i] += (s0[0][i] + s0[1][i]) + s0[2][i]; v[4 + i] += (s1[0][i] + s1[1][i]) + s1[2][i]; }
}

__global__ __launch_bounds__(512, 2) void fused_kernel(const u16* __restrict__ x, const u16* __restrict__ wqkv, const u16* __restrict__ wo,
                                                       const u16* __restrict__ w1, const u16* __restrict__ b1, const u16* __restrict__ w2,
                                                       const u16* __restrict__ b2, const u16* __restrict__ gw, u16* __restrict__ out, char* __restrict__ wsp) {
  extern __shared__ __attribute__((aligned(16))) char smem[];
  const int tid = threadIdx.x, lane = tid & 63;
  const int w = __builtin_amdgcn_readfirstlane(tid >> 6);
  const int b = blockIdx.x, xcd = b & 7, r = b >> 3;
  const int hi = lane >> 5, l31 = lane & 31;
  const int blk = (lane >> 4) & 1, q = (lane & 15) >> 2, p = lane & 3;
  STAMP(0); FSTAMP(60);

  const rsrc_t rX = mkrsrc(x, S * D * 2), rWQ = mkrsrc(wqkv, D * 1536 * 2), rWS = mkrsrc(wsp, WS_USED);
  const rsrc_t rW1 = mkrsrc(w1, S * F * 2), rW2 = mkrsrc(w2, F * S * 2);

  u32 cdummy = 0;
  if (w == 7) {
    u64 pc; u32 len;
    asm volatile("s_getpc_b64 %0\n.Lgm_begin:\n\ts_mov_b32 %1, .Lgm_end-.Lgm_begin" : "=s"(pc), "=s"(len));
    const char* cp = (const char*)(pc & ~127ull);
    for (u32 o = lane * 128; o < len; o += 8192) asm volatile("global_load_dword %0, %1, off" : "+v"(cdummy) : "v"(cp + o));
  }

  const int T = 8 * (xcd >> 1) + (r >> 2), Hq = 4 * (xcd & 1) + (r & 3);
  u32x2 ev; u32x4 xr, gv;
  ld8c(ev, 0, rWS, OFF_EPOCH);
  {
    const int row = 64 * T + 8 * Hq + w;
    ld16p(xr, lane * 16, rX, row * 1024);
    ld16p(gv, lane * 16, mkrsrc(gw, D * 2), 0);
  }
  const u32 vx = (u32)((64 * T + 8 * w + (lane >> 3)) * 1024 + (((lane & 7) ^ ((4 * w + (lane >> 4)) & 7)) * 16));
  const u32 vw = (u32)((8 * w + (lane >> 3)) * 3072 + Hq * 128 + (((lane & 7) ^ (((lane >> 4) & 1) << 2)) * 16));
  const int qstart = ROT ? ((r >> 2) + 2 * (r & 3)) & 7 : 0;
  auto qkv_piece = [&](int c, int i) {
    const int cc = (c + qstart) & 7;
    const u32 sl = (u32)((c & 3) * 32768 + w * 1024 + i * 8192);
    if (i == 0) dma16<0>(sl, vx, rX, cc * 128);
    else dma16<0>(sl, vw, rWQ, cc * 64 * 3072 + (i - 1) * 1024);
  };
  auto qkv_issue = [&](int c) { qkv_piece(c, 0); qkv_piece(c, 1); qkv_piece(c, 2); qkv_piece(c, 3); };
  qkv_issue(0); qkv_issue(1);
  FSTAMP(10);
  asm volatile("s_waitcnt vmcnt(8)" : "+v"(ev), "+v"(xr), "+v"(gv), "+v"(cdummy)::"memory");
  FSTAMP(11);
  const u64 tag = mixtag(((u64)(u32)__builtin_amdgcn_readfirstlane(ev.y) << 32) | (u32)__builtin_amdgcn_readfirstlane(ev.x));
  const u32 tlo = (u32)tag, thi = (u32)(tag >> 32);
  const u32x2 tagv = {tlo, thi};
  {
    float v[8] = {blo(xr.x), bhi(xr.x), blo(xr.y), bhi(xr.y), blo(xr.z), bhi(xr.z), blo(xr.w), bhi(xr.w)};
    float gq[8] = {blo(gv.x), bhi(gv.x), blo(gv.y), bhi(gv.y), blo(gv.z), bhi(gv.z), blo(gv.w), bhi(gv.w)};
    float s = 0.f, gs = 0.f;
#pragma unroll
    for (int j = 0; j < 8; ++j) { s += v[j]; gs += v[j] * gq[j]; }
    wavesum2(s, gs);
    const float mean = s * (1.f / 512.f);
    float ss = 0.f;
#pragma unroll
    for (int j = 0; j < 8; ++j) { v[j] -= mean; ss += v[j] * v[j]; }
    const float rstd = __builtin_amdgcn_rsqf(wavesum(ss) * (1.f / 512.f) + 1e-5f);
    ldsw16(L_LN + w * 1088 + lane * 16, u32x4{cvtpk(v[0] * rstd, v[1] * rstd), cvtpk(v[2] * rstd, v[3] * rstd), cvtpk(v[4] * rstd, v[5] * rstd), cvtpk(v[6] * rstd, v[7] * rstd)});
    const float g = __builtin_amdgcn_rcpf(1.f + expf_(-gs));
    FSTAMP(12);
    lbar();
    FSTAMP(13);
    const u32 la = L_LN + (u32)(q * 1088 + ((tid >> 4) * 16 + 4 * p) * 2);
    const bf16x8 ht = trfrag(la, la + 4 * 1088);
    st16c(__builtin_bit_cast(u32x4, ht), tid * 16, rWS, OFF_HT + (8 * T + Hq) * 8192);
    if (lane == 0) st4c(asu(g), 0, rWS, OFF_G + (64 * T + 8 * Hq + w) * 4);
  }
  qkv_issue(2); qkv_issue(3);
  STAMP(1); FSTAMP(61);

  const int kg = w & 3, dh = w >> 2;
  const u32 swz = (u32)(((lane & 7) ^ (((lane >> 4) & 1) << 2)) * 16);
  const bool isG1 = r < 28;
  const int id1 = xcd * 28 + r, ti = id1 & 7, tj = id1 >> 3;
  const u32 vW1 = (u32)((8 * dh + (lane >> 3)) * 3584 + tj * 128) + swz;
  const int sB = (tj + (ti >> 1)) & 3;
#define G1_ORD(s) (((s) & 12) | (((s) + sB) & 3))
  auto w1_issue = [&](int s) {
    const int ss = G1_ORD(s);
    const u32 sl = (u32)((s & 3) * 16384 + kg * 4096 + dh * 1024);
    const u32 so = (u32)((512 * kg + 32 * ss) * 3584);
    dma16<AUX_W1>(sl, vW1, rW1, so);
    dma16<AUX_W1>(sl + 2048, vW1, rW1, so + 16 * 3584);
  };

  const int th = w & 1, hp = (w >> 1) & 1;
  f32x16 acc0 = {}, acc1 = {};
  u32 ax[4];
  {
    const int row = 32 * th + l31, f = (row >> 1) & 7;
#pragma unroll
    for (int ks = 0; ks < 4; ++ks) ax[ks] = (u32)(row * 128 + (((2 * ks + hi) ^ f) * 16));
  }
  const u32 aw = (u32)(8192 + (8 * hi + q) * 128 + (((4 * hp + 2 * blk + (p >> 1)) ^ (((q >> 1) & 1) << 2)) * 16) + 8 * (p & 1));
  u32x4 fA0 = {tlo, thi, tlo, thi}, fA1 = fA0;
  auto qkv_main = [&](auto kvtag) {
    constexpr bool KV = decltype(kvtag)::value;
    struct QF { bf16x8 x[4], f0[4], f1[4]; };
    QF fa, fb;
    auto rd = [&](int c, QF& f) {
      const u32 sl = (u32)((c & 3) * 32768);
#pragma unroll
      for (int ks = 0; ks < 4; ++ks) {
        f.x[ks] = frag(ldsr16(sl + ax[ks]));
        const u32 a = sl + aw + ks * 2048;
        if constexpr (KV) { f.f0[ks] = trfrag(a + 8192, a + 8192 + 512); f.f1[ks] = trfrag(a + 16384, a + 16384 + 512); }
        else f.f0[ks] = trfrag(a, a + 512);
      }
    };
    auto mma1 = [&](const QF& f, int ks) {
      if constexpr (KV) { acc0 = mfma(f.x[ks], f.f0[ks], acc0); acc1 = mfma(f.x[ks], f.f1[ks], acc1); }
      else acc0 = mfma(f.f0[ks], f.x[ks], acc0);
    };
    auto step = [&](const QF& f, int c) {
#pragma unroll
      for (int ks = 0; ks < 4; ++ks) { mma1(f, ks); SB(); qkv_piece(c, ks); SB(); }
    };
    WAIT_BAR(14); rd(0, fa);
    WAIT_BAR(8); FSTAMP(0); rd(1, fb);
    if (KV && tid == 0) st8c(tagv, 0, rWS, OFF_FA + b * 8);
    SB(); step(fa, 4);
    WAIT_BAR(8); FSTAMP(1); rd(2, fa); SB(); step(fb, 5);
    WAIT_BAR(8); FSTAMP(2); rd(3, fb); SB(); step(fa, 6);
    WAIT_BAR(8); FSTAMP(3); rd(4, fa); SB();
    if (KV && w == 3) { ld16c_rw(fA0, lane * 32, rWS, OFF_FA); ld16c_rw(fA1, lane * 32 + 16, rWS, OFF_FA); }
    step(fb, 7);
    WAIT_BAR(8); FSTAMP(4); rd(5, fb); SB();
    mma1(fa, 0); mma1(fa, 1); SB();
    if (isG1) { w1_issue(0); w1_issue(1); } else { waitvm<0>(); }
    SB(); mma1(fa, 2); mma1(fa, 3);
    WAIT_BAR(8); FSTAMP(5); rd(6, fa); SB();
    mma1(fb, 0); mma1(fb, 1); SB();
    if (KV && w == 3) {
      asm volatile("" : "+v"(fA0), "+v"(fA1));
      for (int it = 0; it < 400000; ++it) {
        const bool ok = fA0.x == tlo && fA0.y == thi && fA0.z == tlo && fA0.w == thi && fA1.x == tlo && fA1.y == thi && fA1.z == tlo && fA1.w == thi;
        if (__all(ok)) break;
        __builtin_amdgcn_s_sleep(1);
        asm volatile("buffer_load_dwordx4 %0, %2, %3, %4 offen sc1\n\tbuffer_load_dwordx4 %1, %2, %3, %4 offen offset:16 sc1\n\ts_waitcnt vmcnt(0)" : "=&v"(fA0), "=&v"(fA1) : "v"(lane * 32), "s"(rWS), "s"((u32)OFF_FA) : "memory");
      }
    }
    if (isG1) w1_issue(2); else waitvm<0>();
    SB(); mma1(fb, 2); mma1(fb, 3);
    WAIT_BAR(6); FSTAMP(6); rd(7, fb); SB();
    mma1(fa, 0); mma1(fa, 1); mma1(fa, 2); mma1(fa, 3);
    FSTAMP(7);
    mma1(fb, 0); mma1(fb, 1); mma1(fb, 2); mma1(fb, 3);
  };
  if (w < 4) qkv_main(std::true_type{}); else qkv_main(std::false_type{});
  const int st = xcd * 4 + (r >> 3), dt = r & 7;
  u32 gS0, gS1, b2S0, b2S1, gT;
  ld4c(gS0, (64 * st + l31) * 4, rWS, OFF_G);
  ld4c(gS1, (64 * st + 32 + l31) * 4, rWS, OFF_G);
  ld4c(gT, (64 * T + 32 * th + l31) * 4, rWS, OFF_G);
  u32x2 b1a, b1b;
  {
    const rsrc_t rB2 = mkrsrc(b2, S * 2), rB1 = mkrsrc(b1, F * 2);
    ld2p(b2S0, (64 * st + l31) * 2, rB2, 0);
    ld2p(b2S1, (64 * st + 32 + l31) * 2, rB2, 0);
    const u32 f0 = (u32)(64 * tj + 32 * (kg >> 1) + 16 * (kg & 1) + 4 * hi) * 2;
    if (isG1) { ld8p(b1a, f0, rB1, 0); ld8p(b1b, f0 + 16, rB1, 0); }
  }
  const u32 vH = (u32)(hi * 8192 + (64 * ti + 32 * dh + l31) * 16);
  u32x4 hr[4][2];
  auto h_issue = [&](int s, u32x4 (&h)[2]) {
    const u32 sh = (u32)(OFF_HT + (32 * kg + 2 * G1_ORD(s)) * 16384);
    ld16c(h[0], vH, rWS, sh);
    ld16c(h[1], vH, rWS, sh + 16384);
  };
  if (isG1) { h_issue(0, hr[0]); h_issue(1, hr[1]); h_issue(2, hr[2]); }
  STAMP(2); FSTAMP(14);
  u32x4 pq;
  {
    f32x4 k0 = {}, k1 = {};
    float zs = 0.f;
    const u32 sc = L_KVR + (u32)(hp * 2304);
    if (w < 4) {
      u32 kb[8], pk[8], vv[8];
#pragma unroll
      for (int i = 0; i < 8; ++i) { kb[i] = cvtpk(acc0[2 * i], acc0[2 * i + 1]); vv[i] = cvtpk(acc1[2 * i], acc1[2 * i + 1]); }
      float pf[16];
#pragma unroll
      for (int i = 0; i < 8; ++i) { pf[2 * i] = phi(blo(kb[i])); pf[2 * i + 1] = phi(bhi(kb[i])); }
#pragma unroll
      for (int i = 0; i < 8; ++i) pk[i] = cvtpk(pf[2 * i], pf[2 * i + 1]);
      f32x16 kvacc = {};
      kvacc = mfma(frag(u32x4{pk[0], pk[1], pk[2], pk[3]}), frag(u32x4{vv[0], vv[1], vv[2], vv[3]}), kvacc);
      kvacc = mfma(frag(u32x4{pk[4], pk[5], pk[6], pk[7]}), frag(u32x4{vv[4], vv[5], vv[6], vv[7]}), kvacc);
      float zp[4];
#pragma unroll
      for (int i = 0; i < 4; ++i) zp[i] = (blo(pk[2 * i]) + bhi(pk[2 * i])) + (blo(pk[2 * i + 1]) + bhi(pk[2 * i + 1]));
      zs = xor32sum((zp[0] + zp[1]) + (zp[2] + zp[3]));
#pragma unroll
      for (int i = 0; i < 4; ++i) {
        const float a = opaque(kvacc[i]), b_ = opaque(kvacc[8 + i]), c = opaque(kvacc[4 + i]), d = opaque(kvacc[12 + i]);
        k0[i] = blk ? b_ : a; k1[i] = blk ? d : c;
      }
      if (th == 1) {
        ldswf4(sc + lane * 32, k0); ldswf4(sc + lane * 32 + 16, k1);
        if (lane < 32) *(LDS float*)(size_t)(sc + 2048 + lane * 4) = zs;
      }
    } else {
      u32 qb[8];
#pragma unroll
      for (int i = 0; i < 8; ++i) qb[i] = cvtpk(acc0[2 * i], acc0[2 * i + 1]);
      float qf[16];
#pragma unroll
      for (int i = 0; i < 8; ++i) { qf[2 * i] = phi(blo(qb[i])); qf[2 * i + 1] = phi(bhi(qb[i])); }
      pq = u32x4{cvtpk(qf[0], qf[1]), cvtpk(qf[2], qf[3]), cvtpk(qf[4], qf[5]), cvtpk(qf[6], qf[7])};
      ldsw16(L_PQX + (w & 3) * 1024 + lane * 16, u32x4{cvtpk(qf[8], qf[9]), cvtpk(qf[10], qf[11]), cvtpk(qf[12], qf[13]), cvtpk(qf[14], qf[15])});
    }
    FSTAMP(15);
    lbar();
    FSTAMP(50);
    if (w < 4) pq = ldsr16(L_PQX + w * 1024 + lane * 16);
    if (w < 4 && th == 0) {
      k0 += ldsrf4(sc + lane * 32); k1 += ldsrf4(sc + lane * 32 + 16);
      zs += *(LDS float*)(size_t)(sc + 2048 + l31 * 4);
      const int head = 4 * Hq + 2 * hp + blk;
      const u32 vo = (u32)(head * 32768 + (hi * 16 + (lane & 15)) * 32);
      st16c(__builtin_bit_cast(u32x4, k0), vo, rWS, OFF_KVP + T * 1024);
      st16c(__builtin_bit_cast(u32x4, k1), vo + 16, rWS, OFF_KVP + T * 1024);
      if (lane < 32) st4c(asu(zs), lane * 4, rWS, OFF_ZP + (Hq * 32 + T) * 256 + hp * 128);
    }
  }
  STAMP(3); FSTAMP(51);

  const u32 vW2 = (u32)((8 * dh + (lane >> 3)) * 4096 + st * 128) + swz;
  const int sC = ((r >> 3) + (r & 7) / 2) & 3;
#define C_ORD(j) ((j) < 12 ? (((j) & 12) | (((j) + sC) & 3)) : (12 | (((j) + sC) & 1)))
  auto w2_issue = [&](int j) {
    const int jj = C_ORD(j);
    const u32 sl = (u32)(((j + 4) % 9) * 16384 + kg * 4096 + dh * 1024);
    const u32 so = (u32)((448 * kg + 32 * jj) * 4096);
    dma16<AUX_W2>(sl, vW2, rW2, so);
    dma16<AUX_W2>(sl + 2048, vW2, rW2, so + 16 * 4096);
  };
  u32x4 zg[4];
  const int qhead = 4 * Hq + 2 * hp + (w < 4 ? 1 : 0);
  const u32 zoff = (u32)(OFF_ZF + qhead * 128);
  auto z_issue = [&]() {
#pragma unroll
    for (int i = 0; i < 4; ++i) ld16c(zg[i], hi * 32 + (i & 1) * 16 + (i >> 1) * 64, rWS, zoff);
  };
  auto pq_scale = [&]() {
    for (int it = 0; it < 400000; ++it) {
      bool ok = true;
#pragma unroll
      for (int i = 0; i < 4; ++i) ok = ok && zg[i].y == tlo && zg[i].w == tlo;
      if (__all(ok)) break;
      __builtin_amdgcn_s_sleep(1);
      asm volatile("buffer_load_dwordx4 %0, %4, %5, %6 offen sc1\n\tbuffer_load_dwordx4 %1, %4, %5, %6 offen offset:16 sc1\n\tbuffer_load_dwordx4 %2, %4, %5, %6 offen offset:64 sc1\n\t"
                   "buffer_load_dwordx4 %3, %4, %5, %6 offen offset:80 sc1\n\ts_waitcnt vmcnt(0)"
                   : "=&v"(zg[0]), "=&v"(zg[1]), "=&v"(zg[2]), "=&v"(zg[3]) : "v"(hi * 32), "s"(rWS), "s"(zoff) : "memory");
    }
    float e[8];
#pragma unroll
    for (int i = 0; i < 4; ++i) { e[2 * i] = blo(pq[i]); e[2 * i + 1] = bhi(pq[i]); }
    float den = (e[0] * asf(zg[0].x) + e[1] * asf(zg[0].z)) + (e[2] * asf(zg[1].x) + e[3] * asf(zg[1].z)) + ((e[4] * asf(zg[2].x) + e[5] * asf(zg[2].z)) + (e[6] * asf(zg[3].x) + e[7] * asf(zg[3].z)));
    den = xor32sum(den) + 1e-6f;
    const float sc = asf(gT) * __builtin_amdgcn_rcpf(den);
    st16c(u32x4{cvtpk(e[0] * sc, e[1] * sc), cvtpk(e[2] * sc, e[3] * sc), cvtpk(e[4] * sc, e[5] * sc), cvtpk(e[6] * sc, e[7] * sc)}, lane * 16, rWS,
          OFF_PQ + (qhead * 64 + 2 * T + th) * 1024);
  };
#define TIE_Z() asm volatile("" : "+v"(zg[0]), "+v"(zg[1]), "+v"(zg[2]), "+v"(zg[3]), "+v"(gT))
  if (b == 0 && tid == 0) st8c(tagv, 0, rWS, OFF_EPOCH);

  if (isG1) {
    const u32 aA0 = (u32)(kg * 4096 + (8 * hi + q) * 128 + (((2 * blk + (p >> 1)) ^ (((q >> 1) & 1) << 2)) * 16) + 8 * (p & 1));
    const u32 aA1 = aA0 ^ 64u;
    f32x16 a0 = {}, a1 = {};
    FSTAMP(52);
    STAMP(4); FSTAMP(54);
#define G1_STAGE(SI)                                                                                              \
  {                                                                                                               \
    asm volatile("s_waitcnt vmcnt(%2) lgkmcnt(0)\n\ts_barrier" : "+v"(hr[(SI) & 3][0]), "+v"(hr[(SI) & 3][1]) : "i"((SI) == 0 ? 4 : (SI) == 1 ? 6 : 8) : "memory"); \
    FSTAMP(16 + (SI));                                                                                            \
    bf16x8 f0[2], f1[2];                                                                                          \
    _Pragma("unroll") for (int ks = 0; ks < 2; ++ks) {                                                            \
      const u32 o = (u32)(((SI) & 3) * 16384 + ks * 2048);                                                        \
      f0[ks] = trfrag(aA0 + o, aA0 + o + 512); f1[ks] = trfrag(aA1 + o, aA1 + o + 512);                           \
    }                                                                                                             \
    if ((SI) == 2 && tid == 0) st8c(tagv, 0, rWS, OFF_FQ + b * 64);                                               \
    if ((SI) + 3 < 16) { w1_issue((SI) + 3); h_issue((SI) + 3, hr[((SI) + 3) & 3]); }                             \
    else if ((SI) < 15) { w2_issue(2 * ((SI) - 13)); w2_issue(2 * ((SI) - 13) + 1); }                             \
    else { w2_issue(4); z_issue(); }                                                                              \
    _Pragma("unroll") for (int ks = 0; ks < 2; ++ks) {                                                            \
      a0 = mfma(f0[ks], frag(hr[(SI) & 3][ks]), a0);                                                              \
      a1 = mfma(f1[ks], frag(hr[(SI) & 3][ks]), a1);                                                              \
    }                                                                                                             \
  }
    G1_STAGE(0) G1_STAGE(1) G1_STAGE(2) G1_STAGE(3) G1_STAGE(4) G1_STAGE(5) G1_STAGE(6) G1_STAGE(7)
    G1_STAGE(8) G1_STAGE(9) G1_STAGE(10) G1_STAGE(11) G1_STAGE(12) G1_STAGE(13) G1_STAGE(14) G1_STAGE(15)
    STAMP(5); FSTAMP(55);
    float v[8];
    rs_put(a0, a1, 0, lane, kg, dh, v);
    lbar();
    rs_get(0, lane, kg, dh, v);
    FSTAMP(56);
    asm volatile("" : "+v"(b1a), "+v"(b1b));
    const float bb[8] = {blo(b1a.x), bhi(b1a.x), blo(b1a.y), bhi(b1a.y), blo(b1b.x), bhi(b1b.x), blo(b1b.y), bhi(b1b.y)};
    float gl[8];
#pragma unroll
    for (int i = 0; i < 8; ++i) gl[i] = gelu(v[i] + bb[i]);
    st16c(u32x4{cvtpk(gl[0], gl[1]), cvtpk(gl[2], gl[3]), cvtpk(gl[4], gl[5]), cvtpk(gl[6], gl[7])}, vH, rWS, OFF_UT + (4 * tj + kg) * 16384);
    waitvm<1>();
    TIE_Z();
    pq_scale();
  } else {
    const int h = xcd * 4 + (r - 28);
    const rsrc_t rWO = mkrsrc(wo, D * D * 2);
    dma16<0>((u32)(2 * w * 1024), lane * 16, rWO, (16 * h + 2 * w) * 1024);
    dma16<0>((u32)((2 * w + 1) * 1024), lane * 16, rWO, (16 * h + 2 * w + 1) * 1024);
    w2_issue(0); w2_issue(1); w2_issue(2); w2_issue(3); w2_issue(4);
    waitvm<10>();
    lbar();
    if (tid == 0) st8c(tagv, 0, rWS, OFF_FQ + b * 64);
    if (w == 0) { const u32 fo = OFF_FQ + blk_qkv(l31, h >> 2) * 64; poll2(rWS, fo, fo, tlo, thi); }
    lbar();
    u32x4 k4[4];
#pragma unroll
    for (int i = 0; i < 4; ++i) ld16c(k4[i], lane * 16, rWS, OFF_KVP + h * 32768 + (4 * w + i) * 1024);
    u32x4 z4[2];
#pragma unroll
    for (int i = 0; i < 2; ++i) ld16c(z4[i], ((lane >> 2) + 16 * i) * 256 + (lane & 3) * 16, rWS, OFF_ZP + (h >> 2) * 8192 + (h & 3) * 64);
    asm volatile("s_waitcnt vmcnt(0)" : "+v"(k4[0]), "+v"(k4[1]), "+v"(k4[2]), "+v"(k4[3]), "+v"(z4[0]), "+v"(z4[1])::"memory");
    f32x4 ks = __builtin_bit_cast(f32x4, k4[0]) + __builtin_bit_cast(f32x4, k4[1]) + __builtin_bit_cast(f32x4, k4[2]) + __builtin_bit_cast(f32x4, k4[3]);
    ldswf4(16384 + w * 1024 + lane * 16, ks);
    if (w == 0) {
      f32x4 zz = __builtin_bit_cast(f32x4, z4[0]) + __builtin_bit_cast(f32x4, z4[1]);
#pragma unroll
      for (int i = 0; i < 4; ++i) {
        float t = zz[i];
        t += __shfl_xor(t, 4); t += __shfl_xor(t, 8); t += __shfl_xor(t, 16); t += __shfl_xor(t, 32);
        zz[i] = t;
      }
      if (lane < 4) ldswf4(25600 + lane * 16, zz);
    }
    lbar();
    if (w == 0) {
      f32x4 t = ldsrf4(16384 + lane * 16);
#pragma unroll
      for (int i = 1; i < 8; ++i) t += ldsrf4(16384 + i * 1024 + lane * 16);
      const int half = lane & 1, e = (lane >> 1) & 15, hl = lane >> 5;
      const u32 pk2 = cvtpk(t[0], t[1]), pk3 = cvtpk(t[2], t[3]);
      const u32 o = 24576 + (8 * half + 4 * hl) * 32 + e * 2;
      *(LDS u16*)(size_t)(o) = (u16)pk2; *(LDS u16*)(size_t)(o + 32) = (u16)(pk2 >> 16);
      *(LDS u16*)(size_t)(o + 64) = (u16)pk3; *(LDS u16*)(size_t)(o + 96) = (u16)(pk3 >> 16);
      if (lane < 16) { const float z = *(LDS float*)(size_t)(25600 + lane * 4); st8c(u32x2{asu(z), tlo}, lane * 8, rWS, OFF_ZF + h * 128); }
    }
    lbar();
    u32x4 kf = ldsr16(24576 + (l31 & 15) * 32 + hi * 16);
    if (l31 >= 16) kf = u32x4{0, 0, 0, 0};
#pragma unroll
    for (int i = 0; i < 2; ++i) {
      const int dtile = 2 * w + i;
      const u32 a = (u32)((8 * hi + q) * 1024 + dtile * 64 + (2 * blk + (p >> 1)) * 16 + 8 * (p & 1));
      const bf16x8 fo = trfrag(a, a + 4096);
      f32x16 m = {};
      m = mfma(frag(kf), fo, m);
      st16c(u32x4{cvtpk(m[0], m[1]), cvtpk(m[2], m[3]), cvtpk(m[4], m[5]), cvtpk(m[6], m[7])}, lane * 16, rWS, OFF_M + (h * 16 + dtile) * 1024);
    }
    waitvm<0>();
    lbar();
    if (tid == 0) st8c(tagv, 0, rWS, OFF_FM + h * 64);
    z_issue();
    waitvm<0>();
    TIE_Z();
    pq_scale();
  }
  asm volatile("" : "+v"(gS0), "+v"(gS1), "+v"(b2S0), "+v"(b2S1));
  FSTAMP(57);
  waitvm<0>();
  FSTAMP(58);
  lbar();
  if (tid == 0) st8c(tagv, 0, rWS, OFF_FU + b * 64);
  STAMP(6); FSTAMP(59);

  w2_issue(5); w2_issue(6); w2_issue(7); w2_issue(8);
  if (w == 0) {
    const u32 fo = OFF_FM + l31 * 64;
    poll2(rWS, fo, fo, tlo, thi);
  }
  lbar();
  u32x4 mr[4][2];
#pragma unroll
  for (int j = 0; j < 4; ++j) {
    const u32 so = (u32)(OFF_M + ((8 * kg + 2 * j) * 16 + 2 * dt + dh) * 1024);
    ld16c(mr[j][0], lane * 16, rWS, so); ld16c(mr[j][1], lane * 16, rWS, so + 16384);
  }
  if (w == 0) {
    const u32 fa = OFF_FU + (lane < 28 ? blk_g1(dt, lane) : blk_qkv(st, lane & 7)) * 64;
    poll2(rWS, fa, fa, tlo, thi);
  }
  lbar();
  STAMP(7); FSTAMP(63);
  {
    const u32 vU = (u32)(hi * 8192 + (64 * dt + 32 * dh + l31) * 16);
    const u32 aB0 = (u32)(kg * 4096 + (4 * hi + q) * 128 + (((2 * blk + (p >> 1)) ^ (((q >> 1) & 1) << 2)) * 16) + 8 * (p & 1));
    const u32 aB1 = aB0 ^ 64u;
    u32x4 ur[4][2];
#define C_ISA(k) ((k) >= 9 && (k) <= 12)
#define C_BST(k) ((k) < 9 ? (k) : (k) - 4)
    auto dir_issue = [&](int k, u32x4 (&u)[2]) {
      const u32 so = (u32)(OFF_UT + (28 * kg + 2 * C_ORD(C_BST(k))) * 16384);
      ld16c(u[0], vU, rWS, so); ld16c(u[1], vU, rWS, so + 16384);
    };
    auto lds_issue = [&](int k) {
      const u32 sl = (u32)(((k + 4) % 9) * 16384 + kg * 4096 + dh * 1024);
      if (C_ISA(k)) {
        const u32 so = (u32)(OFF_PQ + ((8 * kg + 2 * (k - 9)) * 64 + 2 * st + dh) * 1024);
        dma16<16>(sl, lane * 16, rWS, so); dma16<16>(sl + 2048, lane * 16, rWS, so + 65536);
      } else {
        const u32 so = (u32)((448 * kg + 32 * C_ORD(C_BST(k))) * 4096);
        dma16<AUX_W2>(sl, vW2, rW2, so); dma16<AUX_W2>(sl + 2048, vW2, rW2, so + 16 * 4096);
      }
    };
    struct CF { bf16x8 f0[2], f1[2]; };
    CF cf[2];
    auto c_read = [&](int k, CF& f) {
#pragma unroll
      for (int ks = 0; ks < 2; ++ks) {
        const u32 o = (u32)(((k + 4) % 9) * 16384 + ks * 2048);
        if (!C_ISA(k)) { f.f0[ks] = trfrag(aB0 + o, aB0 + o + 1024); f.f1[ks] = trfrag(aB1 + o, aB1 + o + 1024); }
        else { f.f0[ks] = frag(ldsr16(o + kg * 4096 + lane * 16)); f.f1[ks] = frag(ldsr16(o + kg * 4096 + 1024 + lane * 16)); }
      }
    };
    f32x16 a0 = {}, a1 = {}, t0 = {}, t1 = {};
    dir_issue(0, ur[0]); dir_issue(1, ur[1]); dir_issue(2, ur[2]);
    c_read(0, cf[0]);
#define C_L(i) ((i) >= 1 && (i) <= 9 ? 2 : 0)
#define C_D(i) ((i) >= 0 && (i) + 3 <= 17 && !C_ISA((i) + 3) ? 2 : 0)
#define C_OPS(i) (C_L(i) + C_D(i))
#define C_MIN(a, b) ((a) < (b) ? (a) : (b))
#define C_ND(J) (C_ISA(J) ? 63 : (J) < 3 ? 2 * (2 - (J)) + C_OPS((J)-2) + C_OPS((J)-1) : C_L((J)-3) + C_OPS((J)-2) + C_OPS((J)-1))
#define C_NL(J) ((J) + 1 < 9 || (J) + 1 > 17 ? 63 : C_OPS((J)-6) + C_OPS((J)-5) + C_OPS((J)-4) + C_OPS((J)-3) + C_OPS((J)-2) + C_OPS((J)-1))
#define C_STAGE(J)                                                                                                \
  {                                                                                                               \
    asm volatile("s_waitcnt vmcnt(%2) lgkmcnt(0)\n\ts_barrier" : "+v"(ur[(J) & 3][0]), "+v"(ur[(J) & 3][1]) : "i"(C_MIN(C_ND(J), C_NL(J))) : "memory"); \
    FSTAMP(32 + (J));                                                                                             \
    if ((J) + 1 <= 17) c_read((J) + 1, cf[((J) + 1) & 1]);                                                        \
    if (C_D(J)) dir_issue((J) + 3, ur[((J) + 3) & 3]);                                                            \
    if (C_L(J)) lds_issue((J) + 8);                                                                               \
    if ((J) == 9) asm volatile("" : "+v"(mr[0][0]), "+v"(mr[0][1]), "+v"(mr[1][0]), "+v"(mr[1][1]), "+v"(mr[2][0]), "+v"(mr[2][1]), "+v"(mr[3][0]), "+v"(mr[3][1])); \
    _Pragma("unroll") for (int ks = 0; ks < 2; ++ks) {                                                            \
      if (C_ISA(J)) {                                                                                             \
        const bf16x8 d = frag(mr[((J) - 9) & 3][ks]);                                                             \
        t0 = mfma(d, cf[(J) & 1].f0[ks], t0); t1 = mfma(d, cf[(J) & 1].f1[ks], t1);                               \
      } else {                                                                                                    \
        const bf16x8 d = frag(ur[(J) & 3][ks]);                                                                   \
        a0 = mfma(d, cf[(J) & 1].f0[ks], a0); a1 = mfma(d, cf[(J) & 1].f1[ks], a1);                               \
      }                                                                                                           \
    }                                                                                                             \
  }
    C_STAGE(0) C_STAGE(1) C_STAGE(2) C_STAGE(3) C_STAGE(4) C_STAGE(5) C_STAGE(6) C_STAGE(7) C_STAGE(8)
    C_STAGE(9) C_STAGE(10) C_STAGE(11) C_STAGE(12) C_STAGE(13) C_STAGE(14) C_STAGE(15) C_STAGE(16) C_STAGE(17)
    {
      const float s0 = 1.f - asf(gS0), s1 = 1.f - asf(gS1);
#pragma unroll
      for (int i = 0; i < 16; ++i) { a0[i] = a0[i] * s0 + t0[i]; a1[i] = a1[i] * s1 + t1[i]; }
    }
    STAMP(8);
    float v[8];
    rs_put(a0, a1, 65536, lane, kg, dh, v);
    lbar();
    rs_get(65536, lane, kg, dh, v);
    const float gs = (kg >> 1) ? asf(gS1) : asf(gS0);
    const float bias = (1.f - gs) * blo((kg >> 1) ? b2S1 : b2S0);
    u32 a_x = cvtpk(v[0] + bias, v[1] + bias), a_y = cvtpk(v[2] + bias, v[3] + bias), b_x = cvtpk(v[4] + bias, v[5] + bias), b_y = cvtpk(v[6] + bias, v[7] + bias);
    auto s0 = __builtin_amdgcn_permlane32_swap(a_x, b_x, false, false);
    auto s1 = __builtin_amdgcn_permlane32_swap(a_y, b_y, false, false);
    const rsrc_t rO = mkrsrc(out, S * D * 2);
    st16p(u32x4{s0[0], s1[0], s0[1], s1[1]}, (u32)((64 * st + 32 * (kg >> 1) + l31) * 1024 + (64 * dt + 32 * dh + 16 * (kg & 1) + 8 * hi) * 2), rO, 0);
  }
  STAMP(9); FSTAMP(62);
  asm volatile(".Lgm_end:" ::: "memory");
}

extern "C" void kernel_launch(void* const* d_in, const int* in_sizes, int n_in, void* d_out, int out_size, void* d_ws, size_t ws_size, hipStream_t stream) {
  (void)in_sizes; (void)n_in; (void)out_size; (void)ws_size;
  (void)hipFuncSetAttribute((const void*)fused_kernel, hipFuncAttributeMaxDynamicSharedMemorySize, LDS_BYTES);
  fused_kernel<<<256, 512, LDS_BYTES, stream>>>((const u16*)d_in[0], (const u16*)d_in[1], (const u16*)d_in[2], (const u16*)d_in[4], (const u16*)d_in[5],
                                               (const u16*)d_in[6], (const u16*)d_in[7], (const u16*)d_in[8], (u16*)d_out, (char*)d_ws);
}
```

```cpp
#include <hip/hip_runtime.h>
#include <stdint.h>
#include <type_traits>
#pragma clang diagnostic ignored "-Winline-asm"

typedef unsigned short u16;
typedef unsigned int u32;
typedef unsigned long long u64;
typedef __bf16 bf16x8 __attribute__((ext_vector_type(8)));
typedef __bf16 bf16x2 __attribute__((ext_vector_type(2)));
typedef short s16x4 __attribute__((ext_vector_type(4)));
typedef short s16x8 __attribute__((ext_vector_type(8)));
typedef float f32x2 __attribute__((ext_vector_type(2)));
typedef float f32x4 __attribute__((ext_vector_type(4)));
typedef float f32x16 __attribute__((ext_vector_type(16)));
typedef u32 u32x2 __attribute__((ext_vector_type(2)));
typedef u32 u32x4 __attribute__((ext_vector_type(4)));
typedef __amdgpu_buffer_rsrc_t rsrc_t;
#define DI __device__ __forceinline__
#define LDS __attribute__((address_space(3)))

#define STAMPS 0
#define AUX_W1 0
#define AUX_W2 0
#define ROT 1
#define OUT_AUX 0
#define TLBT 0
#define CODEPF 0
#define PFW1 0
#define PFW2 3
#define PFW2_ST 2
#define PF_NT 0
#define XP_USRC 0
#define ORD1 3
#define ORD2 2

constexpr int S = 2048, D = 512, F = 1792;

constexpr u32 OFF_EPOCH = 0;
constexpr u32 OFF_FA = 4096;
constexpr u32 OFF_FQ = OFF_FA + 16384;
constexpr u32 OFF_FU = OFF_FQ + 16384;
constexpr u32 OFF_FM = OFF_FU + 16384;
constexpr u32 OFF_ZF = OFF_FM + 4096;
constexpr u32 OFF_G = 65536;
constexpr u32 OFF_ZP = OFF_G + 8192;
constexpr u32 OFF_KVP = 262144;
constexpr u32 OFF_HT = OFF_KVP + 1048576;
constexpr u32 OFF_PQ = OFF_HT + 2097152;
constexpr u32 OFF_M = OFF_PQ + 2097152;
constexpr u32 OFF_UT = OFF_M + 524288;
constexpr u32 OFF_TS = OFF_UT + 1835008;
constexpr u32 WS_USED = OFF_TS + 4 * 65536;

constexpr u32 L_LN = 131072;
constexpr u32 L_KVR = 147456;
constexpr u32 L_PQX = L_KVR + 4608;
constexpr u32 L_SYNC = 163840 - 64;
constexpr u32 LDS_BYTES = 163840;
constexpr u32 U_EMPTY = 0xFFFFFFFFu;

DI rsrc_t mkrsrc(const void* p, u32 bytes) { return __builtin_amdgcn_make_buffer_rsrc((void*)p, (short)0, (int)bytes, 0x00020000); }
DI u32 cvtpk(float a, float b) { f32x2 v = {a, b}; bf16x2 r = __builtin_convertvector(v, bf16x2); return __builtin_bit_cast(u32, r); }
DI float blo(u32 v) { return __builtin_bit_cast(float, v << 16); }
DI float bhi(u32 v) { return __builtin_bit_cast(float, v & 0xffff0000u); }
DI float asf(u32 v) { return __builtin_bit_cast(float, v); }
DI u32 asu(float v) { return __builtin_bit_cast(u32, v); }
DI bf16x8 frag(u32x4 v) { return __builtin_bit_cast(bf16x8, v); }
DI f32x16 mfma(bf16x8 a, bf16x8 b, f32x16 c) { return __builtin_amdgcn_mfma_f32_32x32x16_bf16(a, b, c, 0, 0, 0); }

template <int AUX> DI void dma16(u32 m0v, u32 voff, rsrc_t r, u32 soff) {
  m0v = __builtin_amdgcn_readfirstlane(m0v);
  if constexpr (AUX == 0) asm volatile("s_mov_b32 m0, %0\n\ts_nop 0\n\tbuffer_load_dwordx4 %1, %2, %3 offen lds" ::"s"(m0v), "v"(voff), "s"(r), "s"(soff) : "memory");
  else if constexpr (AUX == 2) asm volatile("s_mov_b32 m0, %0\n\ts_nop 0\n\tbuffer_load_dwordx4 %1, %2, %3 offen nt lds" ::"s"(m0v), "v"(voff), "s"(r), "s"(soff) : "memory");
  else asm volatile("s_mov_b32 m0, %0\n\ts_nop 0\n\tbuffer_load_dwordx4 %1, %2, %3 offen sc1 lds" ::"s"(m0v), "v"(voff), "s"(r), "s"(soff) : "memory");
}
DI void ld16c(u32x4& d, u32 voff, rsrc_t r, u32 soff) { asm volatile("buffer_load_dwordx4 %0, %1, %2, %3 offen sc1" : "=v"(d) : "v"(voff), "s"(r), "s"(soff)); }
DI void ld16p(u32x4& d, u32 voff, rsrc_t r, u32 soff) { asm volatile("buffer_load_dwordx4 %0, %1, %2, %3 offen" : "=v"(d) : "v"(voff), "s"(r), "s"(soff)); }
DI void ld16c_rw(u32x4& d, u32 voff, rsrc_t r, u32 soff) { asm volatile("buffer_load_dwordx4 %0, %1, %2, %3 offen sc1" : "+v"(d) : "v"(voff), "s"(r), "s"(soff)); }
DI void ld8c(u32x2& d, u32 voff, rsrc_t r, u32 soff) { asm volatile("buffer_load_dwordx2 %0, %1, %2, %3 offen sc1" : "=v"(d) : "v"(voff), "s"(r), "s"(soff)); }
DI void ld8c_rw(u32x2& d, u32 voff, rsrc_t r, u32 soff) { asm volatile("buffer_load_dwordx2 %0, %1, %2, %3 offen sc1" : "+v"(d) : "v"(voff), "s"(r), "s"(soff)); }
DI void ld8p(u32x2& d, u32 voff, rsrc_t r, u32 soff) { asm volatile("buffer_load_dwordx2 %0, %1, %2, %3 offen" : "=v"(d) : "v"(voff), "s"(r), "s"(soff)); }
DI void ld4c(u32& d, u32 voff, rsrc_t r, u32 soff) { asm volatile("buffer_load_dword %0, %1, %2, %3 offen sc1" : "=v"(d) : "v"(voff), "s"(r), "s"(soff)); }
DI void ld2p(u32& d, u32 voff, rsrc_t r, u32 soff) { asm volatile("buffer_load_ushort %0, %1, %2, %3 offen" : "=v"(d) : "v"(voff), "s"(r), "s"(soff)); }
DI void st16c(u32x4 v, u32 voff, rsrc_t r, u32 soff) { asm volatile("buffer_store_dwordx4 %0, %1, %2, %3 offen sc1\n\ts_nop 1" ::"v"(v), "v"(voff), "s"(r), "s"(soff) : "memory"); }
DI void st16p(u32x4 v, u32 voff, rsrc_t r, u32 soff) { asm volatile("buffer_store_dwordx4 %0, %1, %2, %3 offen\n\ts_nop 1" ::"v"(v), "v"(voff), "s"(r), "s"(soff) : "memory"); }
DI void st16o(u32x4 v, u32 voff, rsrc_t r, u32 soff) {
  if (OUT_AUX == 0) asm volatile("buffer_store_dwordx4 %0, %1, %2, %3 offen\n\ts_nop 1" ::"v"(v), "v"(voff), "s"(r), "s"(soff) : "memory");
  else if (OUT_AUX == 16) asm volatile("buffer_store_dwordx4 %0, %1, %2, %3 offen sc1\n\ts_nop 1" ::"v"(v), "v"(voff), "s"(r), "s"(soff) : "memory");
  else if (OUT_AUX == 17) asm volatile("buffer_store_dwordx4 %0, %1, %2, %3 offen sc0 sc1\n\ts_nop 1" ::"v"(v), "v"(voff), "s"(r), "s"(soff) : "memory");
  else asm volatile("buffer_store_dwordx4 %0, %1, %2, %3 offen nt\n\ts_nop 1" ::"v"(v), "v"(voff), "s"(r), "s"(soff) : "memory");
}
DI void touch(u32& d, u32 voff, rsrc_t r, u32 soff) {
  if (PF_NT) asm volatile("buffer_load_dword %0, %1, %2, %3 offen nt" : "+v"(d) : "v"(voff), "s"(r), "s"(soff));
  else asm volatile("buffer_load_dword %0, %1, %2, %3 offen" : "+v"(d) : "v"(voff), "s"(r), "s"(soff));
}
DI void touch_r(u32 voff, rsrc_t r, u32 soff) { asm volatile("buffer_load_dword v255, %0, %1, %2 offen" ::"v"(voff), "s"(r), "s"(soff) : "memory"); }
DI void st8c(u32x2 v, u32 voff, rsrc_t r, u32 soff) { asm volatile("buffer_store_dwordx2 %0, %1, %2, %3 offen sc1" ::"v"(v), "v"(voff), "s"(r), "s"(soff) : "memory"); }
DI void st4c(u32 v, u32 voff, rsrc_t r, u32 soff) { asm volatile("buffer_store_dword %0, %1, %2, %3 offen sc1" ::"v"(v), "v"(voff), "s"(r), "s"(soff) : "memory"); }
template <int N> DI void waitvm() { asm volatile("s_waitcnt vmcnt(%0)" ::"i"(N) : "memory"); }
#define WAIT_BAR(N) asm volatile("s_waitcnt vmcnt(%0) lgkmcnt(0)\n\ts_barrier" ::"i"(N) : "memory")
#define VWAIT_BAR(N) asm volatile("s_waitcnt vmcnt(%0)\n\ts_barrier" ::"i"(N) : "memory")
DI void SB() { __builtin_amdgcn_sched_barrier(0); }
DI void lbar() { asm volatile("s_waitcnt lgkmcnt(0)\n\ts_barrier" ::: "memory"); }

#define RESV "v192", "v193", "v194", "v195", "v196", "v197", "v198", "v199", "v200", "v201", "v202", "v203", "v204", "v205", "v206", "v207", "v208", "v209", "v210", "v211", "v212", "v213", "v214", "v215", "v216", "v217", "v218", "v219", "v220", "v221", "v222", "v223", "v224", "v225", "v226", "v227", "v228", "v229", "v230", "v231", "v232", "v233", "v234", "v235", "v236", "v237", "v238", "v239", "v240", "v241", "v242", "v243", "v244", "v245", "v246", "v247", "v248", "v249", "v250", "v251", "v252", "v253", "v254", "v255"
constexpr int RING = 192, MFR = 224;
template <int A> DI void ldr16c(u32 voff, rsrc_t r, u32 soff) {
  asm volatile("buffer_load_dwordx4 v[%c0:%c1], %2, %3, %4 offen sc1" ::"i"(A), "i"(A + 3), "v"(voff), "s"(r), "s"(soff) : "memory");
}
template <int A> DI void mfma_ra(f32x16& acc, bf16x8 b) {
  asm volatile("s_nop 1\n\tv_mfma_f32_32x32x16_bf16 %0, v[%c2:%c3], %1, %0" : "+v"(acc) : "v"(b), "i"(A), "i"(A + 3));
}
template <int A> DI void mfma_rb(f32x16& acc, bf16x8 a) {
  asm volatile("s_nop 1\n\tv_mfma_f32_32x32x16_bf16 %0, %1, v[%c2:%c3], %0" : "+v"(acc) : "v"(a), "i"(A), "i"(A + 3));
}
DI void mfma_settle(f32x16& a, f32x16& b) { asm volatile("s_nop 15\n\ts_nop 3" : "+v"(a), "+v"(b)); }
template <int A> DI void check_r(u32 voff, rsrc_t r, u32 so0, u32 so1) {
  u32 t0, n;
  asm volatile(
      "s_mov_b32 %1, 0x100000\n"
      "1:\n\t"
      "v_max3_u32 %0, v%c6, v[%c6+1], v[%c6+2]\n\t"
      "v_max3_u32 %0, %0, v[%c6+3], v[%c6+4]\n\t"
      "v_max3_u32 %0, %0, v[%c6+5], v[%c6+6]\n\t"
      "v_max_u32 %0, %0, v[%c6+7]\n\t"
      "v_cmp_eq_u32 vcc, -1, %0\n\t"
      "s_nop 4\n\t"
      "s_cbranch_vccz 2f\n\t"
      "s_sub_u32 %1, %1, 1\n\t"
      "s_cbranch_scc1 2f\n\t"
      "buffer_load_dwordx4 v[%c6:%c6+3], %2, %3, %4 offen sc1\n\t"
      "buffer_load_dwordx4 v[%c6+4:%c6+7], %2, %3, %5 offen sc1\n\t"
      "s_waitcnt vmcnt(0)\n\t"
      "s_branch 1b\n"
      "2:"
      : "=&v"(t0), "=&s"(n)
      : "v"(voff), "s"(r), "s"(so0), "s"(so1), "i"(A)
      : "memory", "vcc", "scc");
}
DI s16x4 trread(u32 a) { return __builtin_amdgcn_ds_read_tr16_b64_v4i16((LDS s16x4*)(size_t)a); }
DI bf16x8 trfrag(u32 a0, u32 a1) { s16x4 lo = trread(a0), hi = trread(a1); return __builtin_bit_cast(bf16x8, (s16x8)__builtin_shufflevector(lo, hi, 0, 1, 2, 3, 4, 5, 6, 7)); }
DI u32x4 ldsr16(u32 a) { return *(LDS u32x4*)(size_t)a; }
DI f32x4 ldsrf4(u32 a) { return *(LDS f32x4*)(size_t)a; }
DI void ldsw16(u32 a, u32x4 v) { *(LDS u32x4*)(size_t)a = v; }
DI void ldswf4(u32 a, f32x4 v) { *(LDS f32x4*)(size_t)a = v; }

template <int CTRL, int RM> DI float dppadd(float v) { return v + __builtin_bit_cast(float, __builtin_amdgcn_update_dpp(0, __builtin_bit_cast(int, v), CTRL, RM, 0xf, false)); }
DI float wavesum(float v) {
  v = dppadd<0x111, 0xf>(v); v = dppadd<0x112, 0xf>(v); v = dppadd<0x114, 0xf>(v); v = dppadd<0x118, 0xf>(v);
  v = dppadd<0x142, 0xa>(v); v = dppadd<0x143, 0xc>(v);
  return __builtin_bit_cast(float, __builtin_amdgcn_readlane(__builtin_bit_cast(int, v), 63));
}
DI void wavesum2(float& a, float& b) {
  a = dppadd<0x111, 0xf>(a); b = dppadd<0x111, 0xf>(b); a = dppadd<0x112, 0xf>(a); b = dppadd<0x112, 0xf>(b);
  a = dppadd<0x114, 0xf>(a); b = dppadd<0x114, 0xf>(b); a = dppadd<0x118, 0xf>(a); b = dppadd<0x118, 0xf>(b);
  a = dppadd<0x142, 0xa>(a); b = dppadd<0x142, 0xa>(b); a = dppadd<0x143, 0xc>(a); b = dppadd<0x143, 0xc>(b);
  a = __builtin_bit_cast(float, __builtin_amdgcn_readlane(__builtin_bit_cast(int, a), 63));
  b = __builtin_bit_cast(float, __builtin_amdgcn_readlane(__builtin_bit_cast(int, b), 63));
}
DI float xor32sum(float v) { auto p = __builtin_amdgcn_permlane32_swap(asu(v), asu(v), false, false); return asf(p[0]) + asf(p[1]); }
DI float expf_(float t) { return __builtin_amdgcn_exp2f(t * 1.44269504089f); }
DI float phi(float t) { return __builtin_fmaxf(t + 1.f, __builtin_amdgcn_exp2f(__builtin_fminf(t * 1.44269504089f, 0.f))) + 1e-6f; }
DI float opaque(float v) { asm volatile("" : "+v"(v)); return v; }
DI float gelu(float t) { float y = 0.7978845608f * (t + 0.044715f * t * t * t); return t * __builtin_amdgcn_rcpf(1.f + __builtin_amdgcn_exp2f(-2.88539008178f * y)); }

DI u64 mixtag(u64 e) { e ^= e >> 29; e *= 0x9E3779B97F4A7C15ull; e ^= e >> 32; e += 0x632BE59BD9B4E019ull; return e; }

DI void poll2(rsrc_t ws, u32 voffA, u32 voffB, u32 tlo, u32 thi) {
  for (int it = 0; it < 400000; ++it) {
    u32x2 a, b;
    asm volatile("buffer_load_dwordx2 %0, %2, %4, 0 offen sc1\n\tbuffer_load_dwordx2 %1, %3, %4, 0 offen sc1\n\ts_waitcnt vmcnt(0)" : "=&v"(a), "=&v"(b) : "v"(voffA), "v"(voffB), "s"(ws) : "memory");
    bool ok = a.x == tlo && a.y == thi && b.x == tlo && b.y == thi;
    if (__all(ok)) break;
    __builtin_amdgcn_s_sleep(1);
  }
}
DI u32 blk_g1(int ti, int tj) { int id = tj * 8 + ti; return (u32)((id % 28) * 8 + id / 28); }
DI u32 blk_qkv(int T, int Hq) { return (u32)((((T & 7) * 4 + (Hq & 3)) * 8) + (T >> 3) * 2 + (Hq >> 2)); }

#define STAMP(i) do {} while (0)
#define FSTAMP(i) do {} while (0)

struct Ctx {
  rsrc_t rW1, rW2, rWS;
  int lane, hi, kg, dh;
};

template <int KG> DI void rs_write(const f32x16& a0, const f32x16& a1, u32 wb, float (&v)[8]) {
#pragma unroll
  for (int p = 0; p < 4; ++p) {
    const f32x16& a = (p >> 1) ? a1 : a0;
    const int o = (p & 1) * 8;
    if (p == KG) {
#pragma unroll
      for (int i = 0; i < 8; ++i) v[i] = a[o + i];
    } else {
      const u32 d = wb + (u32)(p * 6144 + ((KG - p - 1) & 3) * 2048);
      ldswf4(d, f32x4{a[o], a[o + 1], a[o + 2], a[o + 3]});
      ldswf4(d + 1024, f32x4{a[o + 4], a[o + 5], a[o + 6], a[o + 7]});
    }
  }
}
DI void rs_put(const f32x16& a0, const f32x16& a1, u32 base, int lane, int kg, int dh, float (&v)[8]) {
  const u32 wb = base + (u32)(dh * 24576 + lane * 16);
  if (kg == 0) rs_write<0>(a0, a1, wb, v); else if (kg == 1) rs_write<1>(a0, a1, wb, v); else if (kg == 2) rs_write<2>(a0, a1, wb, v); else rs_write<3>(a0, a1, wb, v);
}
DI void rs_get(u32 base, int lane, int kg, int dh, float (&v)[8]) {
  const u32 rb = base + (u32)(dh * 24576 + kg * 6144 + lane * 16);
  f32x4 s0[3], s1[3];
#pragma unroll
  for (int k = 0; k < 3; ++k) { s0[k] = ldsrf4(rb + k * 2048); s1[k] = ldsrf4(rb + k * 2048 + 1024); }
#pragma unroll
  for (int i = 0; i < 4; ++i) { v[i] += (s0[0][i] + s0[1][i]) + s0[2][i]; v[4 + i] += (s1[0][i] + s1[1][i]) + s1[2][i]; }
}

__global__ __launch_bounds__(512, 2) __attribute__((amdgpu_num_vgpr(96))) void fused_kernel(const u16* __restrict__ x, const u16* __restrict__ wqkv, const u16* __restrict__ wo,
                                                       const u16* __restrict__ w1, const u16* __restrict__ b1, const u16* __restrict__ w2,
                                                       const u16* __restrict__ b2, const u16* __restrict__ gw, u16* __restrict__ out, char* __restrict__ wsp) {
  extern __shared__ __attribute__((aligned(16))) char smem[];
  const int tid = threadIdx.x, lane = tid & 63;
  const int w = __builtin_amdgcn_readfirstlane(tid >> 6);
  const int b = blockIdx.x, xcd = b & 7, r = b >> 3;
  const int hi = lane >> 5, l31 = lane & 31;
  const int blk = (lane >> 4) & 1, q = (lane & 15) >> 2, p = lane & 3;
  asm volatile("" ::: RESV);
  STAMP(0); FSTAMP(60);

  const rsrc_t rX = mkrsrc(x, S * D * 2), rWQ = mkrsrc(wqkv, D * 1536 * 2), rWS = mkrsrc(wsp, WS_USED);
  const rsrc_t rW1 = mkrsrc(w1, S * F * 2), rW2 = mkrsrc(w2, F * S * 2);

  u32 cdummy = 0;
  if (CODEPF && w == 7) {
    u64 pc; u32 len;
    asm volatile("s_getpc_b64 %0\n.Lgm_begin:\n\ts_mov_b32 %1, .Lgm_end-.Lgm_begin" : "=s"(pc), "=s"(len));
    const char* cp = (const char*)(pc & ~127ull);
    for (u32 o = lane * 128; o < len; o += 8192) asm volatile("global_load_dword %0, %1, off" : "+v"(cdummy) : "v"(cp + o));
  }

  const int T = 8 * (xcd >> 1) + (r >> 2), Hq = 4 * (xcd & 1) + (r & 3);
  const int kg = w & 3, dh = w >> 2;
  const u32 swz = (u32)(((lane & 7) ^ (((lane >> 4) & 1) << 2)) * 16);
  const bool isG1 = r < 28;
  const int id1 = xcd * 28 + r, ti = id1 & 7, tj = id1 >> 3;
  const u32 vH = (u32)(hi * 8192 + (64 * ti + 32 * dh + l31) * 16);
  u32x2 ev; u32x4 xr, gv;
  ld8c(ev, 0, rWS, OFF_EPOCH);
  {
    const int row = 64 * T + 8 * Hq + w;
    ld16p(xr, lane * 16, rX, row * 1024);
    ld16p(gv, lane * 16, mkrsrc(gw, D * 2), 0);
  }
  const u32 vx = (u32)((64 * T + 8 * w + (lane >> 3)) * 1024 + (((lane & 7) ^ ((4 * w + (lane >> 4)) & 7)) * 16));
  const u32 vw = (u32)((8 * w + (lane >> 3)) * 3072 + Hq * 128 + (((lane & 7) ^ (((lane >> 4) & 1) << 2)) * 16));
  const int qstart = ROT ? ((r >> 2) + 2 * (r & 3)) & 7 : 0;
  auto qkv_piece = [&](int c, int i) {
    const int cc = (c + qstart) & 7;
    const u32 sl = (u32)((c & 3) * 32768 + w * 1024 + i * 8192);
    if (i == 0) dma16<0>(sl, vx, rX, cc * 128);
    else dma16<0>(sl, vw, rWQ, cc * 64 * 3072 + (i - 1) * 1024);
  };
  auto qkv_issue = [&](int c) { qkv_piece(c, 0); qkv_piece(c, 1); qkv_piece(c, 2); qkv_piece(c, 3); };
  qkv_issue(0); qkv_issue(1);
  u32 pdummy = 0;
  auto pf_w1 = [&]() {
#pragma unroll
    for (int i = 0; i < 4; ++i) touch(pdummy, (u32)((64 * r + 16 * i + (lane >> 2)) * 3584 + (lane & 3) * 128), rW1, (u32)(((xcd * 28) >> 3) * 128));
  };
  const u32 pw2 = (u32)((64 * r + (lane >> 2)) * 4096 + (lane & 3) * 128);
  auto pf_w2 = [&]() {
#pragma unroll
    for (int i = 0; i < 4; ++i) touch(pdummy, (u32)((56 * r + 16 * i + (lane >> 2)) * 4096 + (lane & 3) * 128), rW2, (u32)(xcd * 512));
  };
  if (PFW1 == 1 && w == 5) pf_w1();
  if (PFW2 == 1 && w == 6) pf_w2();
  FSTAMP(10);
  asm volatile("s_waitcnt vmcnt(8)" : "+v"(ev), "+v"(xr), "+v"(gv), "+v"(cdummy)::"memory");
  FSTAMP(11);
  const u64 tag = mixtag(((u64)(u32)__builtin_amdgcn_readfirstlane(ev.y) << 32) | (u32)__builtin_amdgcn_readfirstlane(ev.x));
  const u32 tlo = (u32)tag, thi = (u32)(tag >> 32);
  const u32x2 tagv = {tlo, thi};
  {
    float v[8] = {blo(xr.x), bhi(xr.x), blo(xr.y), bhi(xr.y), blo(xr.z), bhi(xr.z), blo(xr.w), bhi(xr.w)};
    float gq[8] = {blo(gv.x), bhi(gv.x), blo(gv.y), bhi(gv.y), blo(gv.z), bhi(gv.z), blo(gv.w), bhi(gv.w)};
    float s = 0.f, gs = 0.f;
#pragma unroll
    for (int j = 0; j < 8; ++j) { s += v[j]; gs += v[j] * gq[j]; }
    wavesum2(s, gs);
    const float mean = s * (1.f / 512.f);
    float ss = 0.f;
#pragma unroll
    for (int j = 0; j < 8; ++j) { v[j] -= mean; ss += v[j] * v[j]; }
    const float rstd = __builtin_amdgcn_rsqf(wavesum(ss) * (1.f / 512.f) + 1e-5f);
    ldsw16(L_LN + w * 1088 + lane * 16, u32x4{cvtpk(v[0] * rstd, v[1] * rstd), cvtpk(v[2] * rstd, v[3] * rstd), cvtpk(v[4] * rstd, v[5] * rstd), cvtpk(v[6] * rstd, v[7] * rstd)});
    const float g = __builtin_amdgcn_rcpf(1.f + expf_(-gs));
    FSTAMP(12);
    lbar();
    FSTAMP(13);
    const u32 la = L_LN + (u32)(q * 1088 + ((tid >> 4) * 16 + 4 * p) * 2);
    const bf16x8 ht = trfrag(la, la + 4 * 1088);
    st16c(__builtin_bit_cast(u32x4, ht), tid * 16, rWS, OFF_HT + (8 * T + Hq) * 8192);
    if (lane == 0) st4c(asu(g), 0, rWS, OFF_G + (64 * T + 8 * Hq + w) * 4);
    if (isG1) st16c(u32x4{U_EMPTY, U_EMPTY, U_EMPTY, U_EMPTY}, vH, rWS, OFF_UT + (4 * tj + kg) * 16384);
  }
  qkv_issue(2); qkv_issue(3);
  STAMP(1); FSTAMP(61);

  const u32 vW1 = (u32)((8 * dh + (lane >> 3)) * 3584 + tj * 128) + swz;
  const int jB = (tj + (ti >> 1)) & 3;
  const u64 ordB = jB == 0 ? 0xfedba9c765832140ull : jB == 1 ? 0xfecbad8769432501ull : jB == 2 ? 0xfdcbe987a5436102ull : 0xedcfa98b65472103ull;
#define G1_ORD(s) ((int)((ordB >> (4 * (s))) & 15))
  auto w1_issue = [&](int s) {
    const int ss = G1_ORD(s);
    const u32 sl = (u32)((s & 3) * 16384 + kg * 4096 + dh * 1024);
    const u32 so = (u32)((512 * kg + 32 * ss) * 3584);
    dma16<AUX_W1>(sl, vW1, rW1, so);
    dma16<AUX_W1>(sl + 2048, vW1, rW1, so + 16 * 3584);
  };

  const int th = w & 1, hp = (w >> 1) & 1;
  f32x16 acc0 = {}, acc1 = {};
  u32 ax[4];
  {
    const int row = 32 * th + l31, f = (row >> 1) & 7;
#pragma unroll
    for (int ks = 0; ks < 4; ++ks) ax[ks] = (u32)(row * 128 + (((2 * ks + hi) ^ f) * 16));
  }
  const u32 aw = (u32)(8192 + (8 * hi + q) * 128 + (((4 * hp + 2 * blk + (p >> 1)) ^ (((q >> 1) & 1) << 2)) * 16) + 8 * (p & 1));
  u32x4 fA0 = {tlo, thi, tlo, thi}, fA1 = fA0;
  auto qkv_main = [&](auto kvtag) {
    constexpr bool KV = decltype(kvtag)::value;
    struct QF { bf16x8 x[4], f0[4], f1[4]; };
    QF fa, fb;
    auto rd = [&](int c, QF& f) {
      const u32 sl = (u32)((c & 3) * 32768);
#pragma unroll
      for (int ks = 0; ks < 4; ++ks) {
        f.x[ks] = frag(ldsr16(sl + ax[ks]));
        const u32 a = sl + aw + ks * 2048;
        if constexpr (KV) { f.f0[ks] = trfrag(a + 8192, a + 8192 + 512); f.f1[ks] = trfrag(a + 16384, a + 16384 + 512); }
        else f.f0[ks] = trfrag(a, a + 512);
      }
    };
    auto mma1 = [&](const QF& f, int ks) {
      if constexpr (KV) { acc0 = mfma(f.x[ks], f.f0[ks], acc0); acc1 = mfma(f.x[ks], f.f1[ks], acc1); }
      else acc0 = mfma(f.f0[ks], f.x[ks], acc0);
    };
    auto step = [&](const QF& f, int c) {
#pragma unroll
      for (int ks = 0; ks < 4; ++ks) { mma1(f, ks); SB(); qkv_piece(c, ks); SB(); }
    };
    WAIT_BAR(14); rd(0, fa);
    WAIT_BAR(8); FSTAMP(0); rd(1, fb);
    if (KV && tid == 0) st8c(tagv, 0, rWS, OFF_FA + b * 8);
    SB(); step(fa, 4);
    WAIT_BAR(8); FSTAMP(1); rd(2, fa); SB(); step(fb, 5);
    WAIT_BAR(8); FSTAMP(2); rd(3, fb); SB(); step(fa, 6);
    WAIT_BAR(8); FSTAMP(3); rd(4, fa); SB();
    if (KV && w == 3) { ld16c_rw(fA0, lane * 32, rWS, OFF_FA); ld16c_rw(fA1, lane * 32 + 16, rWS, OFF_FA); }
    step(fb, 7);
    WAIT_BAR(8); FSTAMP(4); rd(5, fb); SB();
    mma1(fa, 0); mma1(fa, 1); SB();
    if (isG1) { w1_issue(0); w1_issue(1); } else { waitvm<0>(); }
    SB(); mma1(fa, 2); mma1(fa, 3);
    WAIT_BAR(8); FSTAMP(5); rd(6, fa); SB();
    mma1(fb, 0); mma1(fb, 1); SB();
    if (KV && w == 3) {
      asm volatile("" : "+v"(fA0), "+v"(fA1));
      for (int it = 0; it < 400000; ++it) {
        const bool ok = fA0.x == tlo && fA0.y == thi && fA0.z == tlo && fA0.w == thi && fA1.x == tlo && fA1.y == thi && fA1.z == tlo && fA1.w == thi;
        if (__all(ok)) break;
        __builtin_amdgcn_s_sleep(1);
        asm volatile("buffer_load_dwordx4 %0, %2, %3, %4 offen sc1\n\tbuffer_load_dwordx4 %1, %2, %3, %4 offen offset:16 sc1\n\ts_waitcnt vmcnt(0)" : "=&v"(fA0), "=&v"(fA1) : "v"(lane * 32), "s"(rWS), "s"((u32)OFF_FA) : "memory");
      }
    }
    if (isG1) w1_issue(2); else waitvm<0>();
    SB(); mma1(fb, 2); mma1(fb, 3);
    WAIT_BAR(6); FSTAMP(6); rd(7, fb); SB();
    mma1(fa, 0); mma1(fa, 1); mma1(fa, 2); mma1(fa, 3);
    FSTAMP(7);
    mma1(fb, 0); mma1(fb, 1); mma1(fb, 2); mma1(fb, 3);
  };
  if (w < 4) qkv_main(std::true_type{}); else qkv_main(std::false_type{});
  if (PFW2 != 2) asm volatile("" : "+v"(pdummy));
  const int st = xcd * 4 + (r >> 3), dt = r & 7;
  u32 gS0, gS1, b2S0, b2S1, gT;
  ld4c(gS0, (64 * st + l31) * 4, rWS, OFF_G);
  ld4c(gS1, (64 * st + 32 + l31) * 4, rWS, OFF_G);
  ld4c(gT, (64 * T + 32 * th + l31) * 4, rWS, OFF_G);
  u32x2 b1a, b1b;
  {
    const rsrc_t rB2 = mkrsrc(b2, S * 2), rB1 = mkrsrc(b1, F * 2);
    ld2p(b2S0, (64 * st + l31) * 2, rB2, 0);
    ld2p(b2S1, (64 * st + 32 + l31) * 2, rB2, 0);
    const u32 f0 = (u32)(64 * tj + 32 * (kg >> 1) + 16 * (kg & 1) + 4 * hi) * 2;
    if (isG1) { ld8p(b1a, f0, rB1, 0); ld8p(b1b, f0 + 16, rB1, 0); }
  }
#define H_ISSUE(s) do { const u32 sh_ = (u32)(OFF_HT + (32 * kg + 2 * G1_ORD(s)) * 16384); ldr16c<RING + ((s) & 3) * 8>(vH, rWS, sh_); ldr16c<RING + ((s) & 3) * 8 + 4>(vH, rWS, sh_ + 16384); } while (0)
  if (isG1) { H_ISSUE(0); H_ISSUE(1); H_ISSUE(2); }
  STAMP(2); FSTAMP(14);
  u32x4 pq;
  {
    f32x4 k0 = {}, k1 = {};
    float zs = 0.f;
    const u32 sc = L_KVR + (u32)(hp * 2304);
    if (w < 4) {
      u32 kb[8], pk[8], vv[8];
#pragma unroll
      for (int i = 0; i < 8; ++i) { kb[i] = cvtpk(acc0[2 * i], acc0[2 * i + 1]); vv[i] = cvtpk(acc1[2 * i], acc1[2 * i + 1]); }
      float pf[16];
#pragma unroll
      for (int i = 0; i < 8; ++i) { pf[2 * i] = phi(blo(kb[i])); pf[2 * i + 1] = phi(bhi(kb[i])); }
#pragma unroll
      for (int i = 0; i < 8; ++i) pk[i] = cvtpk(pf[2 * i], pf[2 * i + 1]);
      f32x16 kvacc = {};
      kvacc = mfma(frag(u32x4{pk[0], pk[1], pk[2], pk[3]}), frag(u32x4{vv[0], vv[1], vv[2], vv[3]}), kvacc);
      kvacc = mfma(frag(u32x4{pk[4], pk[5], pk[6], pk[7]}), frag(u32x4{vv[4], vv[5], vv[6], vv[7]}), kvacc);
      float zp[4];
#pragma unroll
      for (int i = 0; i < 4; ++i) zp[i] = (blo(pk[2 * i]) + bhi(pk[2 * i])) + (blo(pk[2 * i + 1]) + bhi(pk[2 * i + 1]));
      zs = xor32sum((zp[0] + zp[1]) + (zp[2] + zp[3]));
#pragma unroll
      for (int i = 0; i < 4; ++i) {
        const float a = opaque(kvacc[i]), b_ = opaque(kvacc[8 + i]), c = opaque(kvacc[4 + i]), d = opaque(kvacc[12 + i]);
        k0[i] = blk ? b_ : a; k1[i] = blk ? d : c;
      }
      if (th == 1) {
        ldswf4(sc + lane * 32, k0); ldswf4(sc + lane * 32 + 16, k1);
        if (lane < 32) *(LDS float*)(size_t)(sc + 2048 + lane * 4) = zs;
      }
    } else {
      u32 qb[8];
#pragma unroll
      for (int i = 0; i < 8; ++i) qb[i] = cvtpk(acc0[2 * i], acc0[2 * i + 1]);
      float qf[16];
#pragma unroll
      for (int i = 0; i < 8; ++i) { qf[2 * i] = phi(blo(qb[i])); qf[2 * i + 1] = phi(bhi(qb[i])); }
      pq = u32x4{cvtpk(qf[0], qf[1]), cvtpk(qf[2], qf[3]), cvtpk(qf[4], qf[5]), cvtpk(qf[6], qf[7])};
      ldsw16(L_PQX + (w & 3) * 1024 + lane * 16, u32x4{cvtpk(qf[8], qf[9]), cvtpk(qf[10], qf[11]), cvtpk(qf[12], qf[13]), cvtpk(qf[14], qf[15])});
    }
    FSTAMP(15);
    lbar();
    FSTAMP(50);
    if (w < 4) pq = ldsr16(L_PQX + w * 1024 + lane * 16);
    if (w < 4 && th == 0) {
      k0 += ldsrf4(sc + lane * 32); k1 += ldsrf4(sc + lane * 32 + 16);
      zs += *(LDS float*)(size_t)(sc + 2048 + l31 * 4);
      const int head = 4 * Hq + 2 * hp + blk;
      const u32 vo = (u32)(head * 32768 + (hi * 16 + (lane & 15)) * 32);
      st16c(__builtin_bit_cast(u32x4, k0), vo, rWS, OFF_KVP + T * 1024);
      st16c(__builtin_bit_cast(u32x4, k1), vo + 16, rWS, OFF_KVP + T * 1024);
      if (lane < 32) st4c(asu(zs), lane * 4, rWS, OFF_ZP + (Hq * 32 + T) * 256 + hp * 128);
    }
  }
  STAMP(3); FSTAMP(51);

  const u32 vW2 = (u32)((8 * dh + (lane >> 3)) * 4096 + st * 128) + swz;
  const int jC = ((r >> 3) + (r & 7) / 2) & 3;
  const u64 ordC = jC == 0 ? 0xdba9c765832140ull : jC == 1 ? 0xcbad8769432501ull : jC == 2 ? 0xdcb987a5436102ull : 0xdca98b65472103ull;
#define C_ORD(j) ((int)((ordC >> (4 * (j))) & 15))
  auto w2_issue = [&](int j) {
    const int jj = C_ORD(j);
    const u32 sl = (u32)(((j + 4) % 9) * 16384 + kg * 4096 + dh * 1024);
    const u32 so = (u32)((448 * kg + 32 * jj) * 4096);
    dma16<AUX_W2>(sl, vW2, rW2, so);
    dma16<AUX_W2>(sl + 2048, vW2, rW2, so + 16 * 4096);
  };
  u32x4 zg[4];
  const int qhead = 4 * Hq + 2 * hp + (w < 4 ? 1 : 0);
  const u32 zoff = (u32)(OFF_ZF + qhead * 128);
  auto z_issue = [&]() {
#pragma unroll
    for (int i = 0; i < 4; ++i) ld16c(zg[i], hi * 32 + (i & 1) * 16 + (i >> 1) * 64, rWS, zoff);
  };
  auto pq_scale = [&]() {
    for (int it = 0; it < 400000; ++it) {
      bool ok = true;
#pragma unroll
      for (int i = 0; i < 4; ++i) ok = ok && zg[i].y == tlo && zg[i].w == tlo;
      if (__all(ok)) break;
      __builtin_amdgcn_s_sleep(1);
      asm volatile("buffer_load_dwordx4 %0, %4, %5, %6 offen sc1\n\tbuffer_load_dwordx4 %1, %4, %5, %6 offen offset:16 sc1\n\tbuffer_load_dwordx4 %2, %4, %5, %6 offen offset:64 sc1\n\t"
                   "buffer_load_dwordx4 %3, %4, %5, %6 offen offset:80 sc1\n\ts_waitcnt vmcnt(0)"
                   : "=&v"(zg[0]), "=&v"(zg[1]), "=&v"(zg[2]), "=&v"(zg[3]) : "v"(hi * 32), "s"(rWS), "s"(zoff) : "memory");
    }
    float e[8];
#pragma unroll
    for (int i = 0; i < 4; ++i) { e[2 * i] = blo(pq[i]); e[2 * i + 1] = bhi(pq[i]); }
    float den = (e[0] * asf(zg[0].x) + e[1] * asf(zg[0].z)) + (e[2] * asf(zg[1].x) + e[3] * asf(zg[1].z)) + ((e[4] * asf(zg[2].x) + e[5] * asf(zg[2].z)) + (e[6] * asf(zg[3].x) + e[7] * asf(zg[3].z)));
    den = xor32sum(den) + 1e-6f;
    const float sc = asf(gT) * __builtin_amdgcn_rcpf(den);
    st16c(u32x4{cvtpk(e[0] * sc, e[1] * sc), cvtpk(e[2] * sc, e[3] * sc), cvtpk(e[4] * sc, e[5] * sc), cvtpk(e[6] * sc, e[7] * sc)}, lane * 16, rWS,
          OFF_PQ + (qhead * 64 + 2 * T + th) * 1024);
  };
#define TIE_Z() asm volatile("" : "+v"(zg[0]), "+v"(zg[1]), "+v"(zg[2]), "+v"(zg[3]), "+v"(gT))
  if (b == 0 && tid == 0) st8c(tagv, 0, rWS, OFF_EPOCH);
  u32x2 fM = tagv;

  if (isG1) {
    const u32 aA0 = (u32)(kg * 4096 + (8 * hi + q) * 128 + (((2 * blk + (p >> 1)) ^ (((q >> 1) & 1) << 2)) * 16) + 8 * (p & 1));
    const u32 aA1 = aA0 ^ 64u;
    f32x16 a0 = {}, a1 = {};
    FSTAMP(52);
    STAMP(4); FSTAMP(54);
#define G1_STAGE(SI)                                                                                              \
  {                                                                                                               \
    WAIT_BAR((SI) == 0 ? 4 : (SI) == 1 ? 6 : 8);                                                                  \
    FSTAMP(16 + (SI));                                                                                            \
    bf16x8 f0[2], f1[2];                                                                                          \
    _Pragma("unroll") for (int ks = 0; ks < 2; ++ks) {                                                            \
      const u32 o = (u32)(((SI) & 3) * 16384 + ks * 2048);                                                        \
      f0[ks] = trfrag(aA0 + o, aA0 + o + 512); f1[ks] = trfrag(aA1 + o, aA1 + o + 512);                           \
    }                                                                                                             \
    if ((SI) == 2 && tid == 0) st8c(tagv, 0, rWS, OFF_FQ + b * 64);                                               \
    if (PFW2 == 3 && w == 0 && ((SI) == 2 + jB || (SI) == 6 + jB)) { touch_r(pw2 + ((SI) > 5 + jB ? 32 : 0) * 4096, rW2, xcd * 512); touch_r(pw2 + ((SI) > 5 + jB ? 48 : 16) * 4096, rW2, xcd * 512); } \
    if ((SI) + 3 < 16) w1_issue((SI) + 3);                                                                        \
    else if ((SI) < 15) w2_issue(2 * ((SI) - 13));                                                                \
    else w2_issue(4);                                                                                             \
    mfma_rb<RING + ((SI) & 3) * 8>(a0, f0[0]); mfma_rb<RING + ((SI) & 3) * 8>(a1, f1[0]);                         \
    mfma_rb<RING + ((SI) & 3) * 8 + 4>(a0, f0[1]); mfma_rb<RING + ((SI) & 3) * 8 + 4>(a1, f1[1]);                 \
    if ((SI) + 3 < 16) H_ISSUE((SI) + 3);                                                                         \
    else if ((SI) < 15) w2_issue(2 * ((SI) - 13) + 1);                                                            \
    else z_issue();                                                                                               \
    if ((SI) == 13 && w == 0) ld8c_rw(fM, l31 * 64, rWS, OFF_FM);                                                 \
    SB();                                                                                                         \
  }
    G1_STAGE(0) G1_STAGE(1) G1_STAGE(2) G1_STAGE(3) G1_STAGE(4) G1_STAGE(5) G1_STAGE(6) G1_STAGE(7)
    G1_STAGE(8) G1_STAGE(9) G1_STAGE(10) G1_STAGE(11) G1_STAGE(12) G1_STAGE(13) G1_STAGE(14) G1_STAGE(15)
    mfma_settle(a0, a1);
    STAMP(5); FSTAMP(55);
    if (PFW2 == 2) asm volatile("" : "+v"(pdummy));
    float v[8];
    if (w == 0) {
      asm volatile("s_waitcnt vmcnt(10)" : "+v"(fM)::"memory");
      if (!__all(fM.x == tlo && fM.y == thi)) poll2(rWS, OFF_FM + l31 * 64, OFF_FM + l31 * 64, tlo, thi);
    }
    rs_put(a0, a1, 0, lane, kg, dh, v);
    lbar();
    rs_get(0, lane, kg, dh, v);
    FSTAMP(56);
    asm volatile("" : "+v"(b1a), "+v"(b1b));
    const float bb[8] = {blo(b1a.x), bhi(b1a.x), blo(b1a.y), bhi(b1a.y), blo(b1b.x), bhi(b1b.x), blo(b1b.y), bhi(b1b.y)};
    float gl[8];
#pragma unroll
    for (int i = 0; i < 8; ++i) gl[i] = gelu(v[i] + bb[i]);
    u32x4 uo = {cvtpk(gl[0], gl[1]), cvtpk(gl[2], gl[3]), cvtpk(gl[4], gl[5]), cvtpk(gl[6], gl[7])};
#pragma unroll
    for (int i = 0; i < 4; ++i) uo[i] = uo[i] == U_EMPTY ? 0x7FFF7FFFu : uo[i];
    st16c(uo, vH, rWS, OFF_UT + (4 * tj + kg) * 16384);
    STAMP(6);
    waitvm<1>();
    TIE_Z();
    pq_scale();
  } else {
    const int h = xcd * 4 + (r - 28);
    const rsrc_t rWO = mkrsrc(wo, D * D * 2);
    dma16<0>((u32)(2 * w * 1024), lane * 16, rWO, (16 * h + 2 * w) * 1024);
    dma16<0>((u32)((2 * w + 1) * 1024), lane * 16, rWO, (16 * h + 2 * w + 1) * 1024);
    w2_issue(0); w2_issue(1); w2_issue(2); w2_issue(3); w2_issue(4);
    waitvm<10>();
    lbar();
    if (tid == 0) st8c(tagv, 0, rWS, OFF_FQ + b * 64);
    if (w == 0) { const u32 fo = OFF_FQ + blk_qkv(l31, h >> 2) * 64; poll2(rWS, fo, fo, tlo, thi); }
    lbar();
    u32x4 k4[4];
#pragma unroll
    for (int i = 0; i < 4; ++i) ld16c(k4[i], lane * 16, rWS, OFF_KVP + h * 32768 + (4 * w + i) * 1024);
    u32x4 z4[2];
#pragma unroll
    for (int i = 0; i < 2; ++i) ld16c(z4[i], ((lane >> 2) + 16 * i) * 256 + (lane & 3) * 16, rWS, OFF_ZP + (h >> 2) * 8192 + (h & 3) * 64);
    asm volatile("s_waitcnt vmcnt(0)" : "+v"(k4[0]), "+v"(k4[1]), "+v"(k4[2]), "+v"(k4[3]), "+v"(z4[0]), "+v"(z4[1])::"memory");
    f32x4 ks = __builtin_bit_cast(f32x4, k4[0]) + __builtin_bit_cast(f32x4, k4[1]) + __builtin_bit_cast(f32x4, k4[2]) + __builtin_bit_cast(f32x4, k4[3]);
    ldswf4(16384 + w * 1024 + lane * 16, ks);
    if (w == 0) {
      f32x4 zz = __builtin_bit_cast(f32x4, z4[0]) + __builtin_bit_cast(f32x4, z4[1]);
#pragma unroll
      for (int i = 0; i < 4; ++i) {
        float t = zz[i];
        t += __shfl_xor(t, 4); t += __shfl_xor(t, 8); t += __shfl_xor(t, 16); t += __shfl_xor(t, 32);
        zz[i] = t;
      }
      if (lane < 4) ldswf4(25600 + lane * 16, zz);
    }
    lbar();
    if (w == 0) {
      f32x4 t = ldsrf4(16384 + lane * 16);
#pragma unroll
      for (int i = 1; i < 8; ++i) t += ldsrf4(16384 + i * 1024 + lane * 16);
      const int half = lane & 1, e = (lane >> 1) & 15, hl = lane >> 5;
      const u32 pk2 = cvtpk(t[0], t[1]), pk3 = cvtpk(t[2], t[3]);
      const u32 o = 24576 + (8 * half + 4 * hl) * 32 + e * 2;
      *(LDS u16*)(size_t)(o) = (u16)pk2; *(LDS u16*)(size_t)(o + 32) = (u16)(pk2 >> 16);
      *(LDS u16*)(size_t)(o + 64) = (u16)pk3; *(LDS u16*)(size_t)(o + 96) = (u16)(pk3 >> 16);
      if (lane < 16) { const float z = *(LDS float*)(size_t)(25600 + lane * 4); st8c(u32x2{asu(z), tlo}, lane * 8, rWS, OFF_ZF + h * 128); }
    }
    lbar();
    u32x4 kf = ldsr16(24576 + (l31 & 15) * 32 + hi * 16);
    if (l31 >= 16) kf = u32x4{0, 0, 0, 0};
#pragma unroll
    for (int i = 0; i < 2; ++i) {
      const int dtile = 2 * w + i;
      const u32 a = (u32)((8 * hi + q) * 1024 + dtile * 64 + (2 * blk + (p >> 1)) * 16 + 8 * (p & 1));
      const bf16x8 fo = trfrag(a, a + 4096);
      f32x16 m = {};
      m = mfma(frag(kf), fo, m);
      st16c(u32x4{cvtpk(m[0], m[1]), cvtpk(m[2], m[3]), cvtpk(m[4], m[5]), cvtpk(m[6], m[7])}, lane * 16, rWS, OFF_M + (h * 16 + dtile) * 1024);
    }
    waitvm<0>();
    lbar();
    if (tid == 0) st8c(tagv, 0, rWS, OFF_FM + h * 64);
    z_issue();
    waitvm<0>();
    TIE_Z();
    pq_scale();
    if (w == 0) poll2(rWS, OFF_FM + l31 * 64, OFF_FM + l31 * 64, tlo, thi);
    STAMP(6);
  }
  asm volatile("" : "+v"(gS0), "+v"(gS1), "+v"(b2S0), "+v"(b2S1));
  FSTAMP(57);
  lbar();
  STAMP(7); FSTAMP(63);

  {
    const u32 vU = (u32)(hi * 8192 + (64 * dt + 32 * dh + l31) * 16);
    const u32 aB0 = (u32)(kg * 4096 + (4 * hi + q) * 128 + (((2 * blk + (p >> 1)) ^ (((q >> 1) & 1) << 2)) * 16) + 8 * (p & 1));
    const u32 aB1 = aB0 ^ 64u;
#define C_ISA(k) ((k) >= 11 && (k) <= 14)
#define C_BST(k) ((k) < 11 ? (k) : (k) - 4)
    const int ra = (dt + (r >> 3)) & 3;
    auto u_off = [&](int k) { return (u32)((XP_USRC ? OFF_HT : OFF_UT) + (28 * kg + 2 * C_ORD(C_BST(k))) * 16384); };
#define DIR_ISSUE(k) do { const u32 so_ = u_off(k); ldr16c<RING + ((k) & 3) * 8>(vU, rWS, so_); ldr16c<RING + ((k) & 3) * 8 + 4>(vU, rWS, so_ + 16384); } while (0)
    auto lds_issue = [&](int k) {
      const u32 sl = (u32)(((k + 4) % 9) * 16384 + kg * 4096 + dh * 1024);
      if (C_ISA(k)) {
        const u32 so = (u32)(OFF_PQ + ((8 * kg + 2 * ((k - 11 + ra) & 3)) * 64 + 2 * st + dh) * 1024);
        dma16<16>(sl, lane * 16, rWS, so); dma16<16>(sl + 2048, lane * 16, rWS, so + 65536);
      } else {
        const u32 so = (u32)((448 * kg + 32 * C_ORD(C_BST(k))) * 4096);
        dma16<AUX_W2>(sl, vW2, rW2, so); dma16<AUX_W2>(sl + 2048, vW2, rW2, so + 16 * 4096);
      }
    };
    struct CF { bf16x8 f0[2], f1[2]; };
    CF cf[2];
    auto c_read = [&](int k, CF& f) {
#pragma unroll
      for (int ks = 0; ks < 2; ++ks) {
        const u32 o = (u32)(((k + 4) % 9) * 16384 + ks * 2048);
        if (!C_ISA(k)) { f.f0[ks] = trfrag(aB0 + o, aB0 + o + 1024); f.f1[ks] = trfrag(aB1 + o, aB1 + o + 1024); }
        else { f.f0[ks] = frag(ldsr16(o + kg * 4096 + lane * 16)); f.f1[ks] = frag(ldsr16(o + kg * 4096 + 1024 + lane * 16)); }
      }
    };
    f32x16 a0 = {}, a1 = {}, t0 = {}, t1 = {};
    lds_issue(5); lds_issue(6); lds_issue(7); lds_issue(8);
    auto lds_half = [&](int k, int hf) {
      const u32 sl = (u32)(((k + 4) % 9) * 16384 + kg * 4096 + dh * 1024 + hf * 2048);
      if (C_ISA(k)) dma16<16>(sl, lane * 16, rWS, (u32)(OFF_PQ + ((8 * kg + 2 * ((k - 11 + ra) & 3)) * 64 + 2 * st + dh) * 1024 + hf * 65536));
      else dma16<AUX_W2>(sl, vW2, rW2, (u32)((448 * kg + 32 * C_ORD(C_BST(k)) + 16 * hf) * 4096));
    };
    auto m_off = [&](int i) { return (u32)(OFF_M + ((8 * kg + 2 * ((i + ra) & 3)) * 16 + 2 * dt + dh) * 1024); };
    DIR_ISSUE(0); DIR_ISSUE(1); DIR_ISSUE(2);
    c_read(0, cf[0]);
    u32x2 fP = tagv;
#define C_D(i) ((i) >= 0 && (i) + 3 <= 17 && !C_ISA((i) + 3) ? 2 : 0)
#define C_L(i) ((i) == 1 || (i) == 2 || (i) == 9 || (i) == 10 || (i) == 11 ? 2 : (i) == 7 || (i) == 8 ? 4 : 0)
#define C_M(i) ((i) == 3 || (i) == 4 || (i) == 5 || (i) == 9 ? 2 : 0)
#define C_OPS(i) (C_L(i) + C_D(i) + C_M(i))
#define C_OPS2(a, b) (((a) <= (b) ? C_OPS(a) : 0) + ((a) + 1 <= (b) ? C_OPS((a) + 1) : 0) + ((a) + 2 <= (b) ? C_OPS((a) + 2) : 0) + ((a) + 3 <= (b) ? C_OPS((a) + 3) : 0) + \
                      ((a) + 4 <= (b) ? C_OPS((a) + 4) : 0) + ((a) + 5 <= (b) ? C_OPS((a) + 5) : 0) + ((a) + 6 <= (b) ? C_OPS((a) + 6) : 0) + ((a) + 7 <= (b) ? C_OPS((a) + 7) : 0))
#define C_MIN(a, b) ((a) < (b) ? (a) : (b))
#define C_ND(J) (C_ISA(J) ? 63 : (J) < 3 ? 2 * (2 - (J)) + C_OPS2(0, (J)-1) : C_L((J)-3) + C_M((J)-3) + C_OPS2((J)-2, (J)-1))
#define C_LP(K) ((K) == 9 ? 1 : (K) == 10 ? 2 : (K) == 11 || (K) == 12 ? 7 : (K) == 13 || (K) == 14 ? 8 : (K) == 15 ? 9 : (K) == 16 ? 10 : 11)
#define C_LB(K) (((K) == 11 || (K) == 13 ? 2 : 0) + C_M(C_LP(K)))
#define C_NL(J) ((J) + 1 < 9 || (J) + 1 > 17 ? 63 : C_LB((J) + 1) + C_OPS2(C_LP((J) + 1) + 1, (J)-1))
#define C_NM(J) ((J) == 11 ? C_OPS2(4, 10) : (J) == 12 ? C_OPS2(5, 11) : (J) == 13 ? C_OPS2(6, 12) : (J) == 14 ? C_OPS2(10, 13) : 63)
#define C_STAGE(J)                                                                                                \
  {                                                                                                               \
    VWAIT_BAR(C_MIN(C_MIN(C_ND(J), C_NL(J)), C_NM(J)));                                                                                \
    FSTAMP(32 + (J));                                                                                             \
    if ((J) == 0 && tid == 0) st8c(tagv, 0, rWS, OFF_FU + b * 64);     \
    if ((J) == 3 && w == 7) ld8c_rw(fP, blk_qkv(st, lane & 7) * 64, rWS, OFF_FU);                                 \
               \
    constexpr int LA_ = (J) == 1 ? 9 : (J) == 2 ? 10 : (J) == 7 ? 11 : (J) == 8 ? 13 : (J) == 9 ? 15 : (J) == 10 ? 16 : (J) == 11 ? 17 : 0;    \
    constexpr int RA_ = C_ISA(J) ? MFR + (((J) - 11) & 3) * 8 : RING + ((J) & 3) * 8;                             \
    f32x16& x0_ = C_ISA(J) ? t0 : a0; f32x16& x1_ = C_ISA(J) ? t1 : a1;                                           \
    if (C_D(J)) ldr16c<RING + (((J) + 3) & 3) * 8>(vU, rWS, u_off((J) + 3));                                      \
    if ((J) + 1 <= 17) c_read((J) + 1, cf[((J) + 1) & 1]);                                                        \
    SB();                                                                                                         \
    if (C_D(J)) ldr16c<RING + (((J) + 3) & 3) * 8 + 4>(vU, rWS, u_off((J) + 3) + 16384);                          \
    if (!C_ISA(J)) { const u32 so_ = u_off(J); check_r<RING + ((J) & 3) * 8>(vU, rWS, so_, so_ + 16384); }        \
    mfma_ra<RA_>(x0_, cf[(J) & 1].f0[0]);                                                                         \
    if (LA_) lds_half(LA_, 0);                                                                                    \
    mfma_ra<RA_>(x1_, cf[(J) & 1].f1[0]);                                                                         \
    if (LA_) lds_half(LA_, 1);                                                                                    \
    mfma_ra<RA_ + 4>(x0_, cf[(J) & 1].f0[1]);                                                                     \
    if ((J) == 7 || (J) == 8) lds_half(LA_ + 1, 0);                                                               \
    if (C_M(J)) ldr16c<MFR + ((J) == 9 ? 3 : (J) - 3) * 8>(lane * 16, rWS, m_off((J) == 9 ? 3 : (J) - 3));        \
    mfma_ra<RA_ + 4>(x1_, cf[(J) & 1].f1[1]);                                                                     \
    if ((J) == 7 || (J) == 8) lds_half(LA_ + 1, 1);                                                               \
    if (C_M(J)) ldr16c<MFR + ((J) == 9 ? 3 : (J) - 3) * 8 + 4>(lane * 16, rWS, m_off((J) == 9 ? 3 : (J) - 3) + 16384); \
    if ((J) == 6 && w == 7) {     \
      asm volatile("" : "+v"(fP));                                                                                \
      if (!__all(fP.x == tlo && fP.y == thi)) { const u32 fo = OFF_FU + blk_qkv(st, lane & 7) * 64; poll2(rWS, fo, fo, tlo, thi); } \
    }                                                                                                             \
    SB();                                                                                                         \
  }
    C_STAGE(0) C_STAGE(1) C_STAGE(2) C_STAGE(3) C_STAGE(4) C_STAGE(5) C_STAGE(6) C_STAGE(7) C_STAGE(8)
    C_STAGE(9) C_STAGE(10) C_STAGE(11) C_STAGE(12) C_STAGE(13) C_STAGE(14) C_STAGE(15) C_STAGE(16) C_STAGE(17)
    mfma_settle(a0, a1); mfma_settle(t0, t1);
    {
      const float s0 = 1.f - asf(gS0), s1 = 1.f - asf(gS1);
#pragma unroll
      for (int i = 0; i < 16; ++i) { a0[i] = a0[i] * s0 + t0[i]; a1[i] = a1[i] * s1 + t1[i]; }
    }
    STAMP(8);
    float v[8];
    rs_put(a0, a1, 65536, lane, kg, dh, v);
    lbar();
    rs_get(65536, lane, kg, dh, v);
    const float gs = (kg >> 1) ? asf(gS1) : asf(gS0);
    const float bias = (1.f - gs) * blo((kg >> 1) ? b2S1 : b2S0);
    u32 a_x = cvtpk(v[0] + bias, v[1] + bias), a_y = cvtpk(v[2] + bias, v[3] + bias), b_x = cvtpk(v[4] + bias, v[5] + bias), b_y = cvtpk(v[6] + bias, v[7] + bias);
    auto s0 = __builtin_amdgcn_permlane32_swap(a_x, b_x, false, false);
    auto s1 = __builtin_amdgcn_permlane32_swap(a_y, b_y, false, false);
    const rsrc_t rO = mkrsrc(out, S * D * 2);
    st16o(u32x4{s0[0], s1[0], s0[1], s1[1]}, (u32)((64 * st + 32 * (kg >> 1) + l31) * 1024 + (64 * dt + 32 * dh + 16 * (kg & 1) + 8 * hi) * 2), rO, 0);
  }
  STAMP(9); FSTAMP(62);
  asm volatile(".Lgm_end:" ::: "memory");
}

extern "C" void kernel_launch(void* const* d_in, const int* in_sizes, int n_in, void* d_out, int out_size, void* d_ws, size_t ws_size, hipStream_t stream) {
  (void)in_sizes; (void)n_in; (void)out_size; (void)ws_size;
  (void)hipFuncSetAttribute((const void*)fused_kernel, hipFuncAttributeMaxDynamicSharedMemorySize, LDS_BYTES);
  fused_kernel<<<256, 512, LDS_BYTES, stream>>>((const u16*)d_in[0], (const u16*)d_in[1], (const u16*)d_in[2], (const u16*)d_in[4], (const u16*)d_in[5],
                                               (const u16*)d_in[6], (const u16*)d_in[7], (const u16*)d_in[8], (u16*)d_out, (char*)d_ws);
}
```
